```python
import torch

def _softmax(x, axis=-1):
    return torch.softmax(x, dim=axis)
def _gelu(x):
    return torch.nn.functional.gelu(x, approximate="tanh")
def _silu(x):
    return torch.nn.functional.silu(x)
def _sigmoid(x):
    return torch.sigmoid(x)
def _layernorm(x, axis=-1, eps=1e-5):
    m = x.mean(dim=axis, keepdim=True)
    v = x.var(dim=axis, keepdim=True, unbiased=False)
    return (x - m) / torch.sqrt(v + eps)
def _scatter_add(tgt, idx, src, axis=0):
    return tgt.clone().index_add_(axis, idx, src)
def _segment_sum(src, seg, n):
    out = torch.zeros((n,) + src.shape[1:], dtype=src.dtype)
    return out.index_add_(0, seg, src)
def _pad(x, pw):
    flat = tuple(v for pair in reversed(pw) for v in pair)
    return torch.nn.functional.pad(x, flat)

kernel_name = 'interleave_layers__ssm_conv_h3__swiglu_ffn__prompt'

def setup_inputs():

    _w = (lambda a: (lambda t: t.to(torch.bfloat16) if t.is_floating_point() else t)(torch.as_tensor(a)))
    def _A_setup():
        B, S, D = 1, 2048, 512
        ks = 4
        return {
            "q": _w((lambda *s: torch.randn(*s, dtype=torch.bfloat16) * 0.05)(B, S, D)),
            "k": _w((lambda *s: torch.randn(*s, dtype=torch.bfloat16) * 0.8)(B, S, D)),
            "v": _w((lambda *s: torch.randn(*s, dtype=torch.bfloat16) * 0.8)(B, S, D)),
            "shift_k": _w((lambda *s: torch.randn(*s, dtype=torch.bfloat16) * 0.8)(D, ks) * 0.5),
            "ssm_k": _w((lambda *s: torch.randn(*s, dtype=torch.bfloat16) * 0.8)(D, S) * (1.0 / S ** 0.5)),
            "ssm_D": _w((lambda *s: torch.randn(*s, dtype=torch.bfloat16) * 0.8)(D)),
        }
    _A_in = _A_setup()

    def _B_setup():
        B, S, D, F = 1, 2048, 512, 1376
        return {
            "x": _w((lambda *s: torch.randn(*s, dtype=torch.bfloat16) * 0.05)(B, S, D)),
            "w_gate": _w((lambda *s: torch.randn(*s, dtype=torch.bfloat16) * 0.1)(D, F)),
            "w_up": _w((lambda *s: torch.randn(*s, dtype=torch.bfloat16) * 0.1)(D, F)),
            "w_down": _w((lambda *s: torch.randn(*s, dtype=torch.bfloat16) * 0.1)(F, D)),
        }
    _B_in = _B_setup()

    return {
        "A_q": _A_in["q"],
        "A_k": _A_in["k"],
        "A_v": _A_in["v"],
        "A_shift_k": _A_in["shift_k"],
        "A_ssm_k": _A_in["ssm_k"],
        "A_ssm_D": _A_in["ssm_D"],
        "B_x": _B_in["x"],
        "B_w_gate": _B_in["w_gate"],
        "B_w_up": _B_in["w_up"],
        "B_w_down": _B_in["w_down"],
    }

def reference(A_q, A_k, A_v, A_shift_k, A_ssm_k, A_ssm_D, B_x, B_w_gate, B_w_up, B_w_down):
    def _A_ref(q, k, v, shift_k, ssm_k, ssm_D):
        B, S, D = q.shape
        ks = shift_k.shape[-1]
        k_pad = _pad(k, ((0, 0), (ks - 1, 0), (0, 0)))
        k_shift = torch.zeros_like(k)
        for t in range(ks):
            k_shift = k_shift + k_pad[:, t:t + S, :] * shift_k[:, ks - 1 - t][None, None, :]
        kv = k_shift * v
        L = 2 * S
        kv_pad = _pad(kv, ((0, 0), (0, S), (0, 0)))
        kf_pad = _pad(ssm_k, ((0, 0), (0, S)))
        kv_f = (lambda x, axes=(-2,-1): torch.fft.rfft2(x.float(), dim=axes))(kv_pad[:, None, :, :], axes=(1, 2))
        kf_f = (lambda x, axes=(-2,-1): torch.fft.rfft2(x.float(), dim=axes))(kf_pad[:, None, :], axes=(1, 2))
        kf_f = (lambda x, *d: x.permute(*d))(kf_f, 1, 2, 0)
        y_f = kv_f * kf_f[None, :, :, :]
        y = (lambda x, s, axes=(-2,-1): torch.fft.irfft2(x, s=s, dim=axes).to(torch.bfloat16))(y_f, (1, L), axes=(1, 2))[:, 0, :S, :]
        y = y + kv * ssm_D[None, None, :]
        return q * y

    def _B_ref(x, w_gate, w_up, w_down):
        g = _silu(x @ w_gate)
        u = x @ w_up
        return (g * u) @ w_down

    x = A_q
    x = x + _A_ref(_layernorm(x, axis=-1), A_k, A_v, A_shift_k, A_ssm_k, A_ssm_D)
    x = x + _B_ref(_layernorm(x, axis=-1), B_w_gate, B_w_up, B_w_down)
    x = x + _A_ref(_layernorm(x, axis=-1), A_k, A_v, A_shift_k, A_ssm_k, A_ssm_D)
    x = x + _B_ref(_layernorm(x, axis=-1), B_w_gate, B_w_up, B_w_down)
    return x
```

```cpp
#include <hip/hip_runtime.h>
#include <hip/hip_bf16.h>
#include <stdint.h>

typedef __attribute__((ext_vector_type(8))) short bf16x8;
typedef __attribute__((ext_vector_type(4))) float f32x4;
typedef unsigned short u16;
typedef unsigned int u32;

#define S_LEN 2048
#define D_DIM 512
#define F_DIM 1376
#define NFT 172
#define NKS1 16
#define NKS2 43
#define G_STRIDE 2112

__device__ __forceinline__ float bf2f(u16 v) { return __uint_as_float(((u32)v) << 16); }
__device__ __forceinline__ u16 f2bf(float f) {
  __bf16 b = (__bf16)f;
  return __builtin_bit_cast(u16, b);
}
__device__ __forceinline__ float rbf(float f) { return bf2f(f2bf(f)); }
__device__ __forceinline__ float lo_f(u32 w) { return __uint_as_float(w << 16); }
__device__ __forceinline__ float hi_f(u32 w) { return __uint_as_float(w & 0xffff0000u); }
__device__ __forceinline__ u32 pack2(float a, float b) { return (u32)f2bf(a) | ((u32)f2bf(b) << 16); }

__device__ __forceinline__ bf16x8 as_frag(uint4 v) { return __builtin_bit_cast(bf16x8, v); }

#define PREP_KV_BLOCKS 256
#define PREP_W1_BLOCKS (16 * 22)
#define PREP_W2_BLOCKS (43 * 8)

__global__ __launch_bounds__(256) void k_prep(const u16* __restrict__ kin, const u16* __restrict__ vin,
                                              const u16* __restrict__ shift_k, const u16* __restrict__ wg,
                                              const u16* __restrict__ wu, const u16* __restrict__ wd,
                                              u16* __restrict__ G, u16* __restrict__ Wt, u16* __restrict__ Wdt) {
  __shared__ __attribute__((aligned(16))) u16 sm[2 * 32 * 72];
  const int tid = threadIdx.x;
  const int b = blockIdx.x;
  if (b < PREP_KV_BLOCKS) {
    const int tb = b >> 3, db = b & 7;
    const int t0 = tb * 64, d0 = db * 64;
    const int oct = tid & 7, r = tid >> 3;
    const int t = t0 + 2 * r;
    uint4 kk[5], vv[2], sk[4];
#pragma unroll
    for (int i = 0; i < 5; ++i) {
      int tt = t - 3 + i;
      kk[i] = (tt >= 0) ? *(const uint4*)(kin + (size_t)tt * D_DIM + d0 + oct * 8) : make_uint4(0, 0, 0, 0);
    }
#pragma unroll
    for (int i = 0; i < 2; ++i) vv[i] = *(const uint4*)(vin + (size_t)(t + i) * D_DIM + d0 + oct * 8);
#pragma unroll
    for (int i = 0; i < 4; ++i) sk[i] = *(const uint4*)(shift_k + (size_t)(d0 + oct * 8) * 4 + i * 8);
    const u32* kw = (const u32*)kk;
    const u32* vw = (const u32*)vv;
    const u32* sw = (const u32*)sk;
    u16* T = sm;
#pragma unroll
    for (int c = 0; c < 8; ++c) {
      float w[4];
      w[0] = lo_f(sw[c * 2]); w[1] = hi_f(sw[c * 2]); w[2] = lo_f(sw[c * 2 + 1]); w[3] = hi_f(sw[c * 2 + 1]);
      float kvo[2];
#pragma unroll
      for (int o = 0; o < 2; ++o) {
        float acc = 0.f;
#pragma unroll
        for (int tp = 0; tp < 4; ++tp) {
          u32 kwv = kw[(o + tp) * 4 + (c >> 1)];
          float kval = (c & 1) ? hi_f(kwv) : lo_f(kwv);
          float prod = rbf(kval * w[3 - tp]);
          acc = rbf(acc + prod);
        }
        u32 vwv = vw[o * 4 + (c >> 1)];
        float vval = (c & 1) ? hi_f(vwv) : lo_f(vwv);
        kvo[o] = acc * vval;
      }
      *(u32*)(T + (oct * 8 + c) * 66 + 2 * r) = pack2(kvo[0], kvo[1]);
    }
    __syncthreads();
    const int w = tid >> 6, l = tid & 63;
#pragma unroll 4
    for (int c = 0; c < 16; ++c) {
      int dd = w * 16 + c;
      G[(size_t)(d0 + dd) * G_STRIDE + (2079 - (t0 + l))] = T[dd * 66 + l];
    }
    if (tb == 0 && l < 32) {
      for (int c = 0; c < 16; ++c) G[(size_t)(d0 + w * 16 + c) * G_STRIDE + 2080 + l] = 0;
    }
    if (tb == 31 && l < 32) {
      for (int c = 0; c < 16; ++c) G[(size_t)(d0 + w * 16 + c) * G_STRIDE + l] = 0;
    }
  } else if (b < PREP_KV_BLOCKS + PREP_W1_BLOCKS) {
    const int b2 = b - PREP_KV_BLOCKS;
    const int ks = b2 / 22, fb = b2 % 22;
    const int row = tid >> 3, oct = tid & 7;
    const int f = fb * 64 + oct * 8;
    u16* sG = sm;
    u16* sU = sm + 32 * 72;
    if (f < F_DIM) {
      *(uint4*)(sG + row * 72 + oct * 8) = *(const uint4*)(wg + (size_t)(ks * 32 + row) * F_DIM + f);
      *(uint4*)(sU + row * 72 + oct * 8) = *(const uint4*)(wu + (size_t)(ks * 32 + row) * F_DIM + f);
    }
    __syncthreads();
#pragma unroll
    for (int rep = 0; rep < 2; ++rep) {
      int it = tid + rep * 256;
      int tl = it >> 6, l = it & 63;
      int tile = fb * 8 + tl;
      if (tile < NFT) {
        int rr = l & 15;
        int fl = tl * 8 + (rr >> 1);
        const u16* src = (rr & 1) ? sU : sG;
        int kq = (l >> 4) * 8;
        u32 o[4];
#pragma unroll
        for (int j = 0; j < 4; ++j)
          o[j] = (u32)src[(kq + 2 * j) * 72 + fl] | ((u32)src[(kq + 2 * j + 1) * 72 + fl] << 16);
        *(uint4*)(Wt + ((size_t)(tile * NKS1 + ks) * 64 + l) * 8) = make_uint4(o[0], o[1], o[2], o[3]);
      }
    }
  } else {
    const int b3 = b - PREP_KV_BLOCKS - PREP_W1_BLOCKS;
    const int ks = b3 >> 3, nb = b3 & 7;
    const int row = tid >> 3, oct = tid & 7;
    u16* sD = sm;
    *(uint4*)(sD + row * 72 + oct * 8) = *(const uint4*)(wd + (size_t)(ks * 32 + row) * D_DIM + nb * 64 + oct * 8);
    __syncthreads();
    int nt = tid >> 6, l = tid & 63;
    int n = nt * 16 + (l & 15);
    int kq = (l >> 4) * 8;
    u32 o[4];
#pragma unroll
    for (int j = 0; j < 4; ++j)
      o[j] = (u32)sD[(kq + 2 * j) * 72 + n] | ((u32)sD[(kq + 2 * j + 1) * 72 + n] << 16);
    *(uint4*)(Wdt + ((size_t)((nb * 4 + nt) * NKS2 + ks) * 64 + l) * 8) = make_uint4(o[0], o[1], o[2], o[3]);
  }
}

#define KC_BYTES 4416
#define HI_BYTES 5408
#define CH_BYTES (8 * KC_BYTES + 4 * HI_BYTES)

template <int NT>
__device__ __forceinline__ void conv_steps(const unsigned char* kcb, const unsigned char* hib, int s0, int s1,
                                           f32x4 (&acc)[4][2]) {
  for (int s = s0; s < s1; ++s) {
    bf16x8 bfr[2], afr[NT];
#pragma unroll
    for (int nt = 0; nt < 2; ++nt) bfr[nt] = as_frag(*(const uint4*)(kcb - (16 * nt + 32 * s) * 2));
#pragma unroll
    for (int a = 0; a < NT; ++a) afr[a] = as_frag(*(const uint4*)(hib + (512 * (3 - a) - 32 * s) * 2));
#pragma unroll
    for (int a = 0; a < NT; ++a)
#pragma unroll
      for (int nt = 0; nt < 2; ++nt)
        acc[3 - a][nt] = __builtin_amdgcn_mfma_f32_16x16x32_bf16(afr[a], bfr[nt], acc[3 - a][nt], 0, 0, 0);
  }
}

__global__ __launch_bounds__(512) void k_conv(const u16* __restrict__ G, const u16* __restrict__ ssm_k,
                                              const u16* __restrict__ ssm_D, u16* __restrict__ y) {
  __shared__ __attribute__((aligned(16))) unsigned char smem[2 * CH_BYTES];
  const int tid = threadIdx.x;
  const int ch = tid >> 8, t256 = tid & 255;
  const int d = blockIdx.x * 2 + ch;
  unsigned char* base = smem + ch * CH_BYTES;
  unsigned char* kc = base;
  unsigned char* hi = base + 8 * KC_BYTES;

  {
    const uint4* gsrc = (const uint4*)(G + (size_t)d * G_STRIDE);
    for (int q = t256; q < 263; q += 256) {
      uint4 lo = gsrc[q], hh = gsrc[q + 1];
      u32 W[8] = {lo.x, lo.y, lo.z, lo.w, hh.x, hh.y, hh.z, hh.w};
#pragma unroll
      for (int e = 0; e < 8; ++e) {
        const int sft = 7 - e;
        u32 o[4];
#pragma unroll
        for (int j = 0; j < 4; ++j) {
          if (sft & 1) o[j] = __builtin_amdgcn_alignbit(W[(sft >> 1) + j + 1], W[(sft >> 1) + j], 16);
          else o[j] = W[(sft >> 1) + j];
        }
        *(uint4*)(kc + e * KC_BYTES + q * 16) = make_uint4(o[0], o[1], o[2], o[3]);
      }
    }
  }
  {
    const uint4* hsrc = (const uint4*)(ssm_k + (size_t)d * S_LEN);
    for (int q = t256; q < 324; q += 256) {
      int hq = q - 64;
      uint4 v = (hq >= 0 && hq < 256) ? hsrc[hq] : make_uint4(0, 0, 0, 0);
#pragma unroll
      for (int c = 0; c < 4; ++c) *(uint4*)(hi + c * HI_BYTES + q * 16) = v;
    }
  }
  __syncthreads();

  const int l = tid & 63, wq = (tid >> 6) & 3;
  const int c16 = l & 15, hh = l >> 4;
  const unsigned char* kcb = kc + (c16 & 7) * KC_BYTES + (2072 + 8 * hh - (c16 & 8)) * 2;
  const unsigned char* hib = hi + (c16 >> 2) * HI_BYTES + (512 + 32 * c16 + 8 * hh) * 2;
  f32x4 acc[4][2];
#pragma unroll
  for (int a = 0; a < 4; ++a)
#pragma unroll
    for (int nt = 0; nt < 2; ++nt) acc[a][nt] = (f32x4){0.f, 0.f, 0.f, 0.f};

  const int sb = (wq == 0) ? 0 : (wq == 1) ? 10 : (wq == 2) ? 21 : 36;
  const int se = (wq == 0) ? 10 : (wq == 1) ? 21 : (wq == 2) ? 36 : 65;
  conv_steps<4>(kcb, hib, sb, min(se, 17), acc);
  conv_steps<3>(kcb, hib, max(sb, 17), min(se, 33), acc);
  conv_steps<2>(kcb, hib, max(sb, 33), min(se, 49), acc);
  conv_steps<1>(kcb, hib, max(sb, 49), se, acc);

  __syncthreads();
  float* red = (float*)(base + KC_BYTES);
#pragma unroll
  for (int a = 0; a < 4; ++a)
#pragma unroll
    for (int nt = 0; nt < 2; ++nt)
#pragma unroll
      for (int r = 0; r < 4; ++r)
        red[wq * 2048 + (16 * a + 4 * hh + r) * 32 + nt * 16 + c16] = acc[a][nt][r];
  __syncthreads();
  {
    const int t = tid * 4;
    float res[2][4];
#pragma unroll
    for (int cc = 0; cc < 2; ++cc) {
      const unsigned char* bb = smem + cc * CH_BYTES;
      const float* rd = (const float*)(bb + KC_BYTES);
      f32x4 s = *(const f32x4*)(rd + t);
#pragma unroll
      for (int w = 1; w < 4; ++w) s += *(const f32x4*)(rd + w * 2048 + t);
      float Dv = bf2f(ssm_D[blockIdx.x * 2 + cc]);
      const u16* k0 = (const u16*)bb;
#pragma unroll
      for (int r = 0; r < 4; ++r) {
        float kvv = bf2f(k0[2072 - (t + r)]);
        res[cc][r] = rbf(s[r]) + rbf(kvv * Dv);
      }
    }
#pragma unroll
    for (int r = 0; r < 4; ++r)
      *(u32*)(y + (size_t)(t + r) * D_DIM + blockIdx.x * 2) = pack2(res[0][r], res[1][r]);
  }
}

template <int CTRL>
__device__ __forceinline__ float dpp_add(float x) {
  int v = __builtin_amdgcn_update_dpp(0, __float_as_int(x), CTRL, 0xf, 0xf, true);
  return x + __int_as_float(v);
}
__device__ __forceinline__ float row16_sum(float x) {
  x = dpp_add<0xB1>(x);
  x = dpp_add<0x4E>(x);
  x = dpp_add<0x141>(x);
  x = dpp_add<0x140>(x);
  return x;
}

__device__ __forceinline__ void ln_row(float (&x)[32]) {
  float s = 0.f;
#pragma unroll
  for (int i = 0; i < 32; ++i) s += x[i];
  s = row16_sum(s);
  float mean = s * (1.0f / 512.0f);
  float q = 0.f;
#pragma unroll
  for (int i = 0; i < 32; ++i) { float dlt = x[i] - mean; q += dlt * dlt; }
  q = row16_sum(q);
  float var = rbf(q * (1.0f / 512.0f));
  float mb = rbf(mean);
  float den = rbf(sqrtf(rbf(var + 1e-5f)));
#pragma unroll
  for (int i = 0; i < 32; ++i) x[i] = rbf(rbf(x[i] - mb) / den);
}

#define FFN1_PD 8

template <int NT>
__device__ __forceinline__ void ffn1_body(const u16* __restrict__ xin, const u16* __restrict__ y,
                                          const u16* __restrict__ Wt, u16* __restrict__ xnew,
                                          u16* __restrict__ hT, unsigned char* xs, int rb, int fg, int tile0) {
  const int tid = threadIdx.x;
  const int w = tid >> 6, l = tid & 63;
  const int rr = l >> 4, sub = l & 15;

  uint4 xr[2][4], yr[2][4];
#pragma unroll
  for (int p = 0; p < 2; ++p) {
    size_t rowoff = (size_t)(rb * 64 + w * 8 + p * 4 + rr) * D_DIM + sub * 8;
#pragma unroll
    for (int c = 0; c < 4; ++c) {
      xr[p][c] = *(const uint4*)(xin + rowoff + c * 128);
      yr[p][c] = *(const uint4*)(y + rowoff + c * 128);
    }
  }
  const uint4* wbase[NT];
#pragma unroll
  for (int i = 0; i < NT; ++i) wbase[i] = (const uint4*)(Wt) + (size_t)(tile0 + w + 8 * i) * (NKS1 * 64) + l;
  uint4 wf[FFN1_PD][NT];
#pragma unroll
  for (int s = 0; s < FFN1_PD; ++s)
#pragma unroll
    for (int i = 0; i < NT; ++i) wf[s][i] = wbase[i][s * 64];

#pragma unroll
  for (int p = 0; p < 2; ++p) {
    const int row = w * 8 + p * 4 + rr;
    float xv[32], ln[32];
#pragma unroll
    for (int c = 0; c < 4; ++c) {
      const u32* xw = (const u32*)&xr[p][c];
#pragma unroll
      for (int j = 0; j < 4; ++j) { xv[c * 8 + 2 * j] = lo_f(xw[j]); xv[c * 8 + 2 * j + 1] = hi_f(xw[j]); }
    }
#pragma unroll
    for (int i = 0; i < 32; ++i) ln[i] = xv[i];
    ln_row(ln);
#pragma unroll
    for (int c = 0; c < 4; ++c) {
      const u32* yw = (const u32*)&yr[p][c];
#pragma unroll
      for (int j = 0; j < 4; ++j) {
        xv[c * 8 + 2 * j] = rbf(xv[c * 8 + 2 * j] + rbf(ln[c * 8 + 2 * j] * lo_f(yw[j])));
        xv[c * 8 + 2 * j + 1] = rbf(xv[c * 8 + 2 * j + 1] + rbf(ln[c * 8 + 2 * j + 1] * hi_f(yw[j])));
      }
    }
    if (fg == w) {
      size_t rowoff = (size_t)(rb * 64 + row) * D_DIM + sub * 8;
#pragma unroll
      for (int c = 0; c < 4; ++c) {
        uint4 o;
        o.x = pack2(xv[c * 8 + 0], xv[c * 8 + 1]); o.y = pack2(xv[c * 8 + 2], xv[c * 8 + 3]);
        o.z = pack2(xv[c * 8 + 4], xv[c * 8 + 5]); o.w = pack2(xv[c * 8 + 6], xv[c * 8 + 7]);
        *(uint4*)(xnew + rowoff + c * 128) = o;
      }
    }
    ln_row(xv);
    const int ntile = row >> 4, tok = row & 15;
#pragma unroll
    for (int c = 0; c < 4; ++c) {
      uint4 o;
      o.x = pack2(xv[c * 8 + 0], xv[c * 8 + 1]); o.y = pack2(xv[c * 8 + 2], xv[c * 8 + 3]);
      o.z = pack2(xv[c * 8 + 4], xv[c * 8 + 5]); o.w = pack2(xv[c * 8 + 6], xv[c * 8 + 7]);
      const int kstep = c * 4 + (sub >> 2), chunk = sub & 3;
      *(uint4*)(xs + ((ntile * 16 + kstep) * 64 + chunk * 16 + (tok ^ sub)) * 16) = o;
    }
  }
  __syncthreads();

  f32x4 acc[NT][4];
#pragma unroll
  for (int i = 0; i < NT; ++i)
#pragma unroll
    for (int n = 0; n < 4; ++n) acc[i][n] = (f32x4){0.f, 0.f, 0.f, 0.f};
#pragma unroll
  for (int s = 0; s < NKS1; ++s) {
    bf16x8 xf[4];
    const int swz = sub ^ ((s * 4 + rr) & 15);
#pragma unroll
    for (int n = 0; n < 4; ++n) xf[n] = as_frag(*(const uint4*)(xs + ((n * 16 + s) * 64 + rr * 16 + swz) * 16));
    bf16x8 wcur[NT];
#pragma unroll
    for (int i = 0; i < NT; ++i) wcur[i] = as_frag(wf[s % FFN1_PD][i]);
    if (s + FFN1_PD < NKS1) {
#pragma unroll
      for (int i = 0; i < NT; ++i) wf[s % FFN1_PD][i] = wbase[i][(s + FFN1_PD) * 64];
    }
#pragma unroll
    for (int i = 0; i < NT; ++i)
#pragma unroll
      for (int n = 0; n < 4; ++n)
        acc[i][n] = __builtin_amdgcn_mfma_f32_16x16x32_bf16(wcur[i], xf[n], acc[i][n], 0, 0, 0);
  }

#pragma unroll
  for (int i = 0; i < NT; ++i) {
    const int ft = tile0 + w + 8 * i;
#pragma unroll
    for (int n = 0; n < 4; ++n) {
      float hv[2];
#pragma unroll
      for (int e = 0; e < 2; ++e) {
        float g = rbf(acc[i][n][2 * e]);
        float u = rbf(acc[i][n][2 * e + 1]);
        float sg = rbf(g / (1.0f + __expf(-g)));
        hv[e] = sg * u;
      }
      const int tt = rb * 4 + n;
      *(u32*)(hT + ((size_t)(tt * NFT + ft) * 16 + sub) * 8 + rr * 2) = pack2(hv[0], hv[1]);
    }
  }
}

__global__ __launch_bounds__(512) void k_ffn1(const u16* __restrict__ xin, const u16* __restrict__ y,
                                              const u16* __restrict__ Wt, u16* __restrict__ xnew,
                                              u16* __restrict__ hT) {
  __shared__ __attribute__((aligned(16))) unsigned char xs[64 * 1024];
  const int b = blockIdx.x;
  const int rb = (b & 7) * 4 + ((b >> 3) & 3);
  const int fg = b >> 5;
  const int tile0 = (fg < 4) ? 22 * fg : 88 + 21 * (fg - 4);
  const int ntiles = (fg < 4) ? 22 : 21;
  const int w = __builtin_amdgcn_readfirstlane(threadIdx.x >> 6);
  if (w + 16 < ntiles) ffn1_body<3>(xin, y, Wt, xnew, hT, xs, rb, fg, tile0);
  else ffn1_body<2>(xin, y, Wt, xnew, hT, xs, rb, fg, tile0);
}

#define FFN2_PD 4

template <int NS>
__device__ __forceinline__ void ffn2_body(const uint4* __restrict__ hp, const uint4* __restrict__ wp,
                                          f32x4 (&acc)[4][4]) {
  uint4 hf[FFN2_PD][4], wf[FFN2_PD][4];
#pragma unroll
  for (int s = 0; s < FFN2_PD; ++s)
#pragma unroll
    for (int i = 0; i < 4; ++i) {
      wf[s][i] = wp[(size_t)i * (NKS2 * 64) + s * 64];
      hf[s][i] = hp[(size_t)i * (NFT * 16) + s * 64];
    }
#pragma unroll
  for (int s = 0; s < NS; ++s) {
    bf16x8 hc[4], wc[4];
#pragma unroll
    for (int i = 0; i < 4; ++i) { hc[i] = as_frag(hf[s % FFN2_PD][i]); wc[i] = as_frag(wf[s % FFN2_PD][i]); }
    if (s + FFN2_PD < NS) {
#pragma unroll
      for (int i = 0; i < 4; ++i) {
        wf[s % FFN2_PD][i] = wp[(size_t)i * (NKS2 * 64) + (s + FFN2_PD) * 64];
        hf[s % FFN2_PD][i] = hp[(size_t)i * (NFT * 16) + (s + FFN2_PD) * 64];
      }
    }
#pragma unroll
    for (int m = 0; m < 4; ++m)
#pragma unroll
      for (int n = 0; n < 4; ++n)
        acc[m][n] = __builtin_amdgcn_mfma_f32_16x16x32_bf16(wc[m], hc[n], acc[m][n], 0, 0, 0);
  }
}

__global__ __launch_bounds__(256) void k_ffn2(const u16* __restrict__ hT, const u16* __restrict__ Wdt,
                                              const u16* __restrict__ xres, u16* __restrict__ out) {
  __shared__ __attribute__((aligned(16))) float red[4 * 64 * 68];
  const int b = blockIdx.x;
  const int rb = (b & 7) * 4 + ((b >> 3) & 3);
  const int cb = b >> 5;
  const int tid = threadIdx.x;
  const int w = __builtin_amdgcn_readfirstlane(tid >> 6), l = tid & 63;
  const int ks0 = w * 11;
  const uint4* hp = (const uint4*)hT + (size_t)(rb * 4) * (NFT * 16) + ks0 * 64 + l;
  const uint4* wp = (const uint4*)Wdt + (size_t)(cb * 4) * (NKS2 * 64) + ks0 * 64 + l;
  f32x4 acc[4][4];
#pragma unroll
  for (int m = 0; m < 4; ++m)
#pragma unroll
    for (int n = 0; n < 4; ++n) acc[m][n] = (f32x4){0.f, 0.f, 0.f, 0.f};
  const int tok = tid >> 2, nq = tid & 3;
  const size_t ooff = (size_t)(rb * 64 + tok) * D_DIM + cb * 64 + nq * 16;
  uint4 r0 = *(const uint4*)(xres + ooff), r1 = *(const uint4*)(xres + ooff + 8);

  if (w < 3) ffn2_body<11>(hp, wp, acc);
  else ffn2_body<10>(hp, wp, acc);

  const int c16 = l & 15, hh = l >> 4;
#pragma unroll
  for (int m = 0; m < 4; ++m)
#pragma unroll
    for (int n = 0; n < 4; ++n)
      *(f32x4*)(red + (w * 64 + n * 16 + c16) * 68 + m * 16 + 4 * hh) = acc[m][n];
  __syncthreads();
  float o[16];
#pragma unroll
  for (int q = 0; q < 4; ++q) {
    f32x4 s = *(const f32x4*)(red + tok * 68 + nq * 16 + q * 4);
#pragma unroll
    for (int ww = 1; ww < 4; ++ww) s += *(const f32x4*)(red + (ww * 64 + tok) * 68 + nq * 16 + q * 4);
#pragma unroll
    for (int r = 0; r < 4; ++r) o[q * 4 + r] = rbf(s[r]);
  }
  const u32* rw0 = (const u32*)&r0;
  const u32* rw1 = (const u32*)&r1;
  uint4 o0, o1;
  o0.x = pack2(lo_f(rw0[0]) + o[0], hi_f(rw0[0]) + o[1]);
  o0.y = pack2(lo_f(rw0[1]) + o[2], hi_f(rw0[1]) + o[3]);
  o0.z = pack2(lo_f(rw0[2]) + o[4], hi_f(rw0[2]) + o[5]);
  o0.w = pack2(lo_f(rw0[3]) + o[6], hi_f(rw0[3]) + o[7]);
  o1.x = pack2(lo_f(rw1[0]) + o[8], hi_f(rw1[0]) + o[9]);
  o1.y = pack2(lo_f(rw1[1]) + o[10], hi_f(rw1[1]) + o[11]);
  o1.z = pack2(lo_f(rw1[2]) + o[12], hi_f(rw1[2]) + o[13]);
  o1.w = pack2(lo_f(rw1[3]) + o[14], hi_f(rw1[3]) + o[15]);
  *(uint4*)(out + ooff) = o0;
  *(uint4*)(out + ooff + 8) = o1;
}

extern "C" void kernel_launch(void* const* d_in, const int* in_sizes, int n_in, void* d_out, int out_size,
                              void* d_ws, size_t ws_size, hipStream_t stream) {
  const u16* q = (const u16*)d_in[0];
  const u16* k = (const u16*)d_in[1];
  const u16* v = (const u16*)d_in[2];
  const u16* shift_k = (const u16*)d_in[3];
  const u16* ssm_k = (const u16*)d_in[4];
  const u16* ssm_D = (const u16*)d_in[5];
  const u16* wg = (const u16*)d_in[7];
  const u16* wu = (const u16*)d_in[8];
  const u16* wd = (const u16*)d_in[9];
  u16* out = (u16*)d_out;

  unsigned char* ws = (unsigned char*)d_ws;
  size_t off = 0;
  u16* G = (u16*)(ws + off);   off += (size_t)D_DIM * G_STRIDE * 2;
  u16* Y = (u16*)(ws + off);   off += (size_t)S_LEN * D_DIM * 2;
  u16* Wt = (u16*)(ws + off);  off += (size_t)NFT * NKS1 * 1024;
  u16* Wdt = (u16*)(ws + off); off += (size_t)32 * NKS2 * 1024;
  u16* hT = (u16*)(ws + off);  off += (size_t)128 * NFT * 256;
  u16* Xa = (u16*)(ws + off);  off += (size_t)S_LEN * D_DIM * 2;
  u16* Xb = (u16*)(ws + off);  off += (size_t)S_LEN * D_DIM * 2;

  k_prep<<<PREP_KV_BLOCKS + PREP_W1_BLOCKS + PREP_W2_BLOCKS, 256, 0, stream>>>(k, v, shift_k, wg, wu, wd, G, Wt, Wdt);
  k_conv<<<256, 512, 0, stream>>>(G, ssm_k, ssm_D, Y);
  k_ffn1<<<256, 512, 0, stream>>>(q, Y, Wt, Xa, hT);
  k_ffn2<<<256, 256, 0, stream>>>(hT, Wdt, Xa, Xb);
  k_ffn1<<<256, 512, 0, stream>>>(Xb, Y, Wt, Xa, hT);
  k_ffn2<<<256, 256, 0, stream>>>(hT, Wdt, Xa, out);
}
```

```cpp
#include <hip/hip_runtime.h>
#include <hip/hip_bf16.h>
#include <stdint.h>

typedef __attribute__((ext_vector_type(8))) short bf16x8;
typedef __attribute__((ext_vector_type(4))) float f32x4;
typedef unsigned short u16;
typedef unsigned int u32;

#define STAMP(k, i)

#define S_LEN 2048
#define D_DIM 512
#define F_DIM 1376
#define NFT 172
#define NKS1 16
#define NKS2 43
#define G_STRIDE 2112

__device__ __forceinline__ float bf2f(u16 v) { return __uint_as_float(((u32)v) << 16); }
__device__ __forceinline__ u16 f2bf(float f) {
  __bf16 b = (__bf16)f;
  return __builtin_bit_cast(u16, b);
}
__device__ __forceinline__ float rbf(float f) { return bf2f(f2bf(f)); }
__device__ __forceinline__ float lo_f(u32 w) { return __uint_as_float(w << 16); }
__device__ __forceinline__ float hi_f(u32 w) { return __uint_as_float(w & 0xffff0000u); }
__device__ __forceinline__ u32 pack2(float a, float b) { return (u32)f2bf(a) | ((u32)f2bf(b) << 16); }

__device__ __forceinline__ bf16x8 as_frag(uint4 v) { return __builtin_bit_cast(bf16x8, v); }

__global__ __launch_bounds__(256) void k_prep(const u16* __restrict__ kin, const u16* __restrict__ vin,
                                              const u16* __restrict__ shift_k, u16* __restrict__ G,
                                              unsigned* __restrict__ cnt) {
  __shared__ __attribute__((aligned(16))) u16 T[64 * 34];
  const int tid = threadIdx.x;
  const int b = blockIdx.x;
  STAMP(0, 0);
  if (b == 0) {
#pragma unroll
    for (int i = 0; i < 4; ++i) cnt[tid + i * 256] = 0u;
  }
  const int tb = b >> 3, db = b & 7;
  const int t0 = tb * 32, d0 = db * 64;
  const int oct = tid & 7, r = tid >> 3;
  const int t = t0 + r;
  uint4 kk[4], vv, sk[4];
#pragma unroll
  for (int i = 0; i < 4; ++i) {
    int tt = t - 3 + i;
    kk[i] = (tt >= 0) ? *(const uint4*)(kin + (size_t)tt * D_DIM + d0 + oct * 8) : make_uint4(0, 0, 0, 0);
  }
  vv = *(const uint4*)(vin + (size_t)t * D_DIM + d0 + oct * 8);
#pragma unroll
  for (int i = 0; i < 4; ++i) sk[i] = *(const uint4*)(shift_k + (size_t)(d0 + oct * 8) * 4 + i * 8);
  const u32* kw = (const u32*)kk;
  const u32* vw = (const u32*)&vv;
  const u32* sw = (const u32*)sk;
#pragma unroll
  for (int c = 0; c < 8; ++c) {
    float w[4];
    w[0] = lo_f(sw[c * 2]); w[1] = hi_f(sw[c * 2]); w[2] = lo_f(sw[c * 2 + 1]); w[3] = hi_f(sw[c * 2 + 1]);
    float acc = 0.f;
#pragma unroll
    for (int tp = 0; tp < 4; ++tp) {
      u32 kwv = kw[tp * 4 + (c >> 1)];
      float kval = (c & 1) ? hi_f(kwv) : lo_f(kwv);
      float prod = rbf(kval * w[3 - tp]);
      acc = rbf(acc + prod);
    }
    float vval = (c & 1) ? hi_f(vw[c >> 1]) : lo_f(vw[c >> 1]);
    T[(oct * 8 + c) * 34 + r] = f2bf(acc * vval);
  }
  __syncthreads();
  const int hw = tid >> 5, l32 = tid & 31;
#pragma unroll
  for (int c = 0; c < 8; ++c) {
    int dd = hw * 8 + c;
    G[(size_t)(d0 + dd) * G_STRIDE + (2079 - (t0 + l32))] = T[dd * 34 + l32];
  }
  if (tb == 0) {
#pragma unroll
    for (int c = 0; c < 8; ++c) G[(size_t)(d0 + hw * 8 + c) * G_STRIDE + 2080 + l32] = 0;
  }
  if (tb == 63) {
#pragma unroll
    for (int c = 0; c < 8; ++c) G[(size_t)(d0 + hw * 8 + c) * G_STRIDE + l32] = 0;
  }
  STAMP(0, 1);
}

#define KC_BYTES 4416
#define HI_BYTES 5408
#define CH_BYTES (8 * KC_BYTES + 4 * HI_BYTES)
#define RT_OFF (2 * CH_BYTES)
#define CONV_LDS (RT_OFF + 2 * 3 * 32 * 72 * 2)
#define W1_UNITS (16 * 22)
#define W2_UNITS (43 * 8)

#define CONV_PD 4
__device__ __forceinline__ constexpr int conv_nt(int s) { return 4 - (s >= 17) - (s >= 33) - (s >= 49); }

template <int S0, int S1>
__device__ __forceinline__ void conv_range(const unsigned char* kcb, const unsigned char* hib, f32x4 (&acc)[4][2]) {
  uint4 bb[CONV_PD][2], aa[CONV_PD][4];
#pragma unroll
  for (int p = 0; p < CONV_PD - 1; ++p) {
    const int s = S0 + p;
    if (s < S1) {
#pragma unroll
      for (int nt = 0; nt < 2; ++nt) bb[p][nt] = *(const uint4*)(kcb - (16 * nt + 32 * s) * 2);
#pragma unroll
      for (int a = 0; a < 4; ++a)
        if (a < conv_nt(s)) aa[p][a] = *(const uint4*)(hib + (512 * (3 - a) - 32 * s) * 2);
    }
  }
  __builtin_amdgcn_sched_barrier(0);
#pragma unroll
  for (int s = S0; s < S1; ++s) {
    const int sn = s + CONV_PD - 1;
    if (sn < S1) {
      const int p = (sn - S0) % CONV_PD;
#pragma unroll
      for (int nt = 0; nt < 2; ++nt) bb[p][nt] = *(const uint4*)(kcb - (16 * nt + 32 * sn) * 2);
#pragma unroll
      for (int a = 0; a < 4; ++a)
        if (a < conv_nt(sn)) aa[p][a] = *(const uint4*)(hib + (512 * (3 - a) - 32 * sn) * 2);
    }
    __builtin_amdgcn_sched_barrier(0);
    const int p = (s - S0) % CONV_PD;
#pragma unroll
    for (int a = 0; a < 4; ++a)
      if (a < conv_nt(s)) {
#pragma unroll
        for (int nt = 0; nt < 2; ++nt)
          acc[3 - a][nt] = __builtin_amdgcn_mfma_f32_16x16x32_bf16(as_frag(bb[p][nt]), as_frag(aa[p][a]), acc[3 - a][nt], 0, 0, 0);
      }
    __builtin_amdgcn_sched_barrier(0);
  }
}

__global__ __launch_bounds__(512) void k_conv(const u16* __restrict__ G, const u16* __restrict__ ssm_k,
                                              const u16* __restrict__ ssm_D, u16* __restrict__ y,
                                              const u16* __restrict__ wg, const u16* __restrict__ wu,
                                              const u16* __restrict__ wd, u16* __restrict__ Wt,
                                              u16* __restrict__ Wdt) {
  __shared__ __attribute__((aligned(16))) unsigned char smem[CONV_LDS];
  const int tid = threadIdx.x;
  const int ch = tid >> 8, t256 = tid & 255;
  const int pb = (blockIdx.x & 7) * 32 + (blockIdx.x >> 3);
  const int d = pb * 2 + ch;
  unsigned char* base = smem + ch * CH_BYTES;
  unsigned char* kc = base;
  unsigned char* hi = base + 8 * KC_BYTES;

  STAMP(1, 0);
  const uint4* gsrc = (const uint4*)(G + (size_t)d * G_STRIDE);
  const uint4* hsrc = (const uint4*)(ssm_k + (size_t)d * S_LEN);
  uint4 g0 = gsrc[t256], g1 = gsrc[t256 + 1];
  uint4 g2 = make_uint4(0, 0, 0, 0), g3 = make_uint4(0, 0, 0, 0);
  if (t256 < 7) { g2 = gsrc[256 + t256]; g3 = gsrc[257 + t256]; }
  uint4 h0 = hsrc[t256];

  const int rrow = t256 >> 3, roct = t256 & 7;
  uint4 wl[2][2];
#pragma unroll
  for (int rnd = 0; rnd < 2; ++rnd) {
    const int u = blockIdx.x + rnd * 256;
    wl[rnd][0] = make_uint4(0, 0, 0, 0); wl[rnd][1] = make_uint4(0, 0, 0, 0);
    if (ch == 0) {
      const int ks = u / 22, fb = u % 22;
      const int f = fb * 64 + roct * 8;
      if (u < W1_UNITS && f < F_DIM) {
        wl[rnd][0] = *(const uint4*)(wg + (size_t)(ks * 32 + rrow) * F_DIM + f);
        wl[rnd][1] = *(const uint4*)(wu + (size_t)(ks * 32 + rrow) * F_DIM + f);
      }
    } else {
      if (u < W2_UNITS) {
        const int ks = u >> 3, nb = u & 7;
        wl[rnd][0] = *(const uint4*)(wd + (size_t)(ks * 32 + rrow) * D_DIM + nb * 64 + roct * 8);
      }
    }
  }

#pragma unroll
  for (int rep = 0; rep < 2; ++rep) {
    const int q = t256 + rep * 256;
    if (rep == 0 || t256 < 7) {
      uint4 lo = rep ? g2 : g0, hh = rep ? g3 : g1;
      u32 W[8] = {lo.x, lo.y, lo.z, lo.w, hh.x, hh.y, hh.z, hh.w};
#pragma unroll
      for (int e = 0; e < 8; ++e) {
        const int sft = 7 - e;
        u32 o[4];
#pragma unroll
        for (int j = 0; j < 4; ++j) {
          if (sft & 1) o[j] = __builtin_amdgcn_alignbit(W[(sft >> 1) + j + 1], W[(sft >> 1) + j], 16);
          else o[j] = W[(sft >> 1) + j];
        }
        *(uint4*)(kc + e * KC_BYTES + q * 16) = make_uint4(o[0], o[1], o[2], o[3]);
      }
    }
  }
  {
#pragma unroll
    for (int c = 0; c < 4; ++c) *(uint4*)(hi + c * HI_BYTES + (64 + t256) * 16) = h0;
    if (t256 < 68) {
      const int q = (t256 < 64) ? t256 : (256 + t256);
#pragma unroll
      for (int c = 0; c < 4; ++c) *(uint4*)(hi + c * HI_BYTES + q * 16) = make_uint4(0, 0, 0, 0);
    }
  }
  STAMP(1, 1);
  __syncthreads();
  STAMP(1, 2);
  const int l = tid & 63, wq = (tid >> 6) & 3;
  const int c16 = l & 15, hh = l >> 4;
  const unsigned char* kcb = kc + (c16 & 7) * KC_BYTES + (2072 + 8 * hh - (c16 & 8)) * 2;
  const unsigned char* hib = hi + (c16 >> 2) * HI_BYTES + (512 + 32 * c16 + 8 * hh) * 2;
  f32x4 acc[4][2];
#pragma unroll
  for (int a = 0; a < 4; ++a)
#pragma unroll
    for (int nt = 0; nt < 2; ++nt) acc[a][nt] = (f32x4){0.f, 0.f, 0.f, 0.f};

  {
    const int wqu = __builtin_amdgcn_readfirstlane(wq);
    if (wqu == 0) conv_range<0, 10>(kcb, hib, acc);
    else if (wqu == 1) conv_range<10, 21>(kcb, hib, acc);
    else if (wqu == 2) conv_range<21, 36>(kcb, hib, acc);
    else conv_range<36, 65>(kcb, hib, acc);
  }

  u16* sc = (u16*)(smem + RT_OFF);
#pragma unroll
  for (int rnd = 0; rnd < 2; ++rnd) {
    u16* scr = sc + rnd * (3 * 32 * 72);
    if (ch == 0) {
      *(uint4*)(scr + rrow * 72 + roct * 8) = wl[rnd][0];
      *(uint4*)(scr + 32 * 72 + rrow * 72 + roct * 8) = wl[rnd][1];
    } else {
      *(uint4*)(scr + 64 * 72 + rrow * 72 + roct * 8) = wl[rnd][0];
    }
  }
  STAMP(1, 3);
  __syncthreads();
  STAMP(1, 4);
  float* red = (float*)(base + KC_BYTES);
#pragma unroll
  for (int a = 0; a < 4; ++a)
#pragma unroll
    for (int nt = 0; nt < 2; ++nt)
      *(f32x4*)(red + wq * 2304 + (16 * a + c16) * 36 + nt * 16 + 4 * hh) = acc[a][nt];
  __syncthreads();
#pragma unroll
  for (int rnd = 0; rnd < 2; ++rnd) {
    const int u = blockIdx.x + rnd * 256;
    const u16* scr = sc + rnd * (3 * 32 * 72);
    if (ch == 0) {
      if (u < W1_UNITS) {
        const int ks = u / 22, fb = u % 22;
#pragma unroll
        for (int rep = 0; rep < 2; ++rep) {
          int it = t256 + rep * 256;
          int tl = it >> 6, l = it & 63;
          int tile = fb * 8 + tl;
          if (tile < NFT) {
            int rr = l & 15;
            int fl = tl * 8 + (rr >> 1);
            const u16* src = scr + (rr & 1) * 32 * 72;
            int kq = (l >> 4) * 8;
            u32 o[4];
#pragma unroll
            for (int j = 0; j < 4; ++j)
              o[j] = (u32)src[(kq + 2 * j) * 72 + fl] | ((u32)src[(kq + 2 * j + 1) * 72 + fl] << 16);
            *(uint4*)(Wt + ((size_t)(tile * NKS1 + ks) * 64 + l) * 8) = make_uint4(o[0], o[1], o[2], o[3]);
          }
        }
      }
    } else {
      if (u < W2_UNITS) {
        const int ks = u >> 3, nb = u & 7;
        const u16* src = scr + 64 * 72;
        int nt = t256 >> 6, l = t256 & 63;
        int n = nt * 16 + (l & 15);
        int kq = (l >> 4) * 8;
        u32 o[4];
#pragma unroll
        for (int j = 0; j < 4; ++j)
          o[j] = (u32)src[(kq + 2 * j) * 72 + n] | ((u32)src[(kq + 2 * j + 1) * 72 + n] << 16);
        *(uint4*)(Wdt + ((size_t)((nb * 4 + nt) * NKS2 + ks) * 64 + l) * 8) = make_uint4(o[0], o[1], o[2], o[3]);
      }
    }
  }
  {
    const int t = tid * 4;
    const int ro = (t >> 5) * 36 + (t & 31);
    float res[2][4];
#pragma unroll
    for (int cc = 0; cc < 2; ++cc) {
      const unsigned char* bb = smem + cc * CH_BYTES;
      const float* rd = (const float*)(bb + KC_BYTES);
      f32x4 s = *(const f32x4*)(rd + ro);
#pragma unroll
      for (int w = 1; w < 4; ++w) s += *(const f32x4*)(rd + w * 2304 + ro);
      float Dv = bf2f(ssm_D[pb * 2 + cc]);
      const u16* k0 = (const u16*)bb;
#pragma unroll
      for (int r = 0; r < 4; ++r) {
        float kvv = bf2f(k0[2072 - (t + r)]);
        res[cc][r] = rbf(s[r]) + rbf(kvv * Dv);
      }
    }
#pragma unroll
    for (int r = 0; r < 4; ++r)
      *(u32*)(y + (size_t)(t + r) * D_DIM + pb * 2) = pack2(res[0][r], res[1][r]);
  }
  STAMP(1, 5);
}

template <int CTRL>
__device__ __forceinline__ float dpp_add(float x) {
  int v = __builtin_amdgcn_update_dpp(0, __float_as_int(x), CTRL, 0xf, 0xf, true);
  return x + __int_as_float(v);
}
__device__ __forceinline__ float row16_sum(float x) {
  x = dpp_add<0xB1>(x);
  x = dpp_add<0x4E>(x);
  x = dpp_add<0x141>(x);
  x = dpp_add<0x140>(x);
  return x;
}

typedef __attribute__((ext_vector_type(2))) float f32x2;
typedef __attribute__((ext_vector_type(2))) __bf16 bf16x2_t;
__device__ __forceinline__ u32 cvt_pk(float a, float b) {
  f32x2 v = {a, b};
  bf16x2_t r = __builtin_convertvector(v, bf16x2_t);
  return __builtin_bit_cast(u32, r);
}
__device__ __forceinline__ void ln_stats(const u32 (&xp)[16], float& inv, float& nmi) {
  const bf16x2_t one = __builtin_bit_cast(bf16x2_t, 0x3f803f80u);
  float s0 = 0.f, s1 = 0.f, q0 = 0.f, q1 = 0.f;
#pragma unroll
  for (int i = 0; i < 16; i += 2) {
    bf16x2_t a = __builtin_bit_cast(bf16x2_t, xp[i]), b = __builtin_bit_cast(bf16x2_t, xp[i + 1]);
    s0 = __builtin_amdgcn_fdot2_f32_bf16(a, one, s0, false);
    q0 = __builtin_amdgcn_fdot2_f32_bf16(a, a, q0, false);
    s1 = __builtin_amdgcn_fdot2_f32_bf16(b, one, s1, false);
    q1 = __builtin_amdgcn_fdot2_f32_bf16(b, b, q1, false);
  }
  float mean = row16_sum(s0 + s1) * (1.0f / 512.0f);
  float ex2 = row16_sum(q0 + q1) * (1.0f / 512.0f);
  float var = fmaxf(ex2 - mean * mean, 0.f);
  inv = __builtin_amdgcn_rsqf(var + 1e-5f);
  nmi = -mean * inv;
}

#define FFN1_PD 6
#define AUX_SC1 16
#define RSRC_W3 0x00020000
typedef unsigned v4u __attribute__((ext_vector_type(4)));
typedef __attribute__((address_space(1))) unsigned gu32;
#define RLX_AGENT __ATOMIC_RELAXED, __HIP_MEMORY_SCOPE_AGENT
#define SPIN_CAP (1u << 20)

__device__ __forceinline__ uint4 ld_sc1(__amdgpu_buffer_rsrc_t rs, int off) {
  v4u v = __builtin_amdgcn_raw_buffer_load_b128(rs, off, 0, AUX_SC1);
  return make_uint4(v.x, v.y, v.z, v.w);
}
__device__ __forceinline__ void st_sc1(__amdgpu_buffer_rsrc_t rs, int off, uint4 v) {
  v4u t = {v.x, v.y, v.z, v.w};
  __builtin_amdgcn_raw_buffer_store_b128(t, rs, off, 0, AUX_SC1);
}
__device__ __forceinline__ void publish(gu32* cnt) {
  asm volatile("s_waitcnt vmcnt(0)" ::: "memory");
  __syncthreads();
  if (threadIdx.x == 0) __hip_atomic_fetch_add(cnt, 1u, RLX_AGENT);
}
__device__ __forceinline__ void wait_for(gu32* cnt, unsigned want) {
  if (threadIdx.x == 0) {
    for (unsigned spins = 0; spins < SPIN_CAP; ++spins) {
      if (__hip_atomic_load(cnt, RLX_AGENT) >= want) break;
      __builtin_amdgcn_s_sleep(1);
    }
    __builtin_amdgcn_fence(__ATOMIC_ACQUIRE, "wavefront");
  }
  __syncthreads();
}

#define XS_BYTES 65536
#define RED_BYTES (8 * 64 * 68 * 4)
#define XRES_OFF RED_BYTES
#define FFN_LDS (RED_BYTES + 64 * 144)

template <int NT, bool XSC1>
__device__ __forceinline__ void ffn_phase_a(const u16* __restrict__ xin, __amdgpu_buffer_rsrc_t rsx,
                                            const uint4 (&yr)[2][4], const u16* __restrict__ Wt,
                                            __amdgpu_buffer_rsrc_t rsh, unsigned char* lds, int rb, int fg, int tile0) {
  const int tid = threadIdx.x;
  const int w = tid >> 6, l = tid & 63;
  const int rr = l >> 4, sub = l & 15;
  unsigned char* xs = lds;

  uint4 xr[2][4];
#pragma unroll
  for (int p = 0; p < 2; ++p) {
    const int rowoff = (rb * 64 + w * 8 + p * 4 + rr) * D_DIM + sub * 8;
#pragma unroll
    for (int c = 0; c < 4; ++c) {
      if (XSC1) xr[p][c] = ld_sc1(rsx, (rowoff + c * 128) * 2);
      else xr[p][c] = *(const uint4*)(xin + rowoff + c * 128);
    }
  }
  __builtin_amdgcn_sched_barrier(0);
  const uint4* wbase[NT];
#pragma unroll
  for (int i = 0; i < NT; ++i) wbase[i] = (const uint4*)(Wt) + (size_t)(tile0 + w + 8 * i) * (NKS1 * 64) + l;
  uint4 wf[FFN1_PD][NT];
#pragma unroll
  for (int s = 0; s < 2; ++s)
#pragma unroll
    for (int i = 0; i < NT; ++i) wf[s][i] = wbase[i][s * 64];
  __builtin_amdgcn_sched_barrier(0);
#pragma unroll
  for (int p = 0; p < 2; ++p) {
    const int row = w * 8 + p * 4 + rr;
    u32 xp[16];
#pragma unroll
    for (int c = 0; c < 4; ++c) { xp[c * 4] = xr[p][c].x; xp[c * 4 + 1] = xr[p][c].y; xp[c * 4 + 2] = xr[p][c].z; xp[c * 4 + 3] = xr[p][c].w; }
    float inv, nmi;
    ln_stats(xp, inv, nmi);
    f32x2 inv2 = {inv, inv}, nmi2 = {nmi, nmi};
    f32x2 rv[16];
#pragma unroll
    for (int c = 0; c < 4; ++c) {
      const u32* yw = (const u32*)&yr[p][c];
#pragma unroll
      for (int j = 0; j < 4; ++j) {
        f32x2 xv = {lo_f(xp[c * 4 + j]), hi_f(xp[c * 4 + j])};
        f32x2 yv = {lo_f(yw[j]), hi_f(yw[j])};
        f32x2 ln = xv * inv2 + nmi2;
        rv[c * 4 + j] = ln * yv + xv;
        xp[c * 4 + j] = cvt_pk(rv[c * 4 + j].x, rv[c * 4 + j].y);
      }
    }
    {
      const int cc = fg >> 1, s0 = (fg & 1) * 8;
      if (sub >= s0 && sub < s0 + 8) {
        uint4 o = make_uint4(0, 0, 0, 0);
#pragma unroll
        for (int c = 0; c < 4; ++c)
          if (c == cc) o = make_uint4(xp[c * 4], xp[c * 4 + 1], xp[c * 4 + 2], xp[c * 4 + 3]);
        *(uint4*)(lds + XRES_OFF + row * 144 + (sub - s0) * 16) = o;
      }
    }
    ln_stats(xp, inv, nmi);
    inv2 = (f32x2){inv, inv}; nmi2 = (f32x2){nmi, nmi};
    const int ntile = row >> 4, tok = row & 15;
#pragma unroll
    for (int c = 0; c < 4; ++c) {
      u32 o[4];
#pragma unroll
      for (int j = 0; j < 4; ++j) { f32x2 r = rv[c * 4 + j] * inv2 + nmi2; o[j] = cvt_pk(r.x, r.y); }
      const int kstep = c * 4 + (sub >> 2), chunk = sub & 3;
      *(uint4*)(xs + ((ntile * 16 + kstep) * 64 + chunk * 16 + (tok ^ sub)) * 16) = make_uint4(o[0], o[1], o[2], o[3]);
    }
    __builtin_amdgcn_sched_barrier(0);
#pragma unroll
    for (int s = 2 + 2 * p; s < 4 + 2 * p; ++s)
#pragma unroll
      for (int i = 0; i < NT; ++i) wf[s][i] = wbase[i][s * 64];
    __builtin_amdgcn_sched_barrier(0);
  }
  STAMP(2, 2);
  __syncthreads();
  STAMP(2, 3);
  f32x4 acc[NT][4];
#pragma unroll
  for (int i = 0; i < NT; ++i)
#pragma unroll
    for (int n = 0; n < 4; ++n) acc[i][n] = (f32x4){0.f, 0.f, 0.f, 0.f};
#pragma unroll
  for (int s = 0; s < NKS1; ++s) {
    bf16x8 xf[4];
    const int swz = sub ^ ((s * 4 + rr) & 15);
#pragma unroll
    for (int n = 0; n < 4; ++n) xf[n] = as_frag(*(const uint4*)(xs + ((n * 16 + s) * 64 + rr * 16 + swz) * 16));
#pragma unroll
    for (int i = 0; i < NT; ++i)
#pragma unroll
      for (int n = 0; n < 4; ++n)
        acc[i][n] = __builtin_amdgcn_mfma_f32_16x16x32_bf16(as_frag(wf[s % FFN1_PD][i]), xf[n], acc[i][n], 0, 0, 0);
    __builtin_amdgcn_sched_barrier(0);
    if (s + FFN1_PD < NKS1) {
#pragma unroll
      for (int i = 0; i < NT; ++i) wf[s % FFN1_PD][i] = wbase[i][(s + FFN1_PD) * 64];
    }
    __builtin_amdgcn_sched_barrier(0);
  }
  STAMP(2, 4);
#pragma unroll
  for (int i = 0; i < NT; ++i) {
    const int ft = tile0 + w + 8 * i;
    u32 r[4];
#pragma unroll
    for (int n = 0; n < 4; ++n) {
      float hv[2];
#pragma unroll
      for (int e = 0; e < 2; ++e) {
        float g = acc[i][n][2 * e];
        float u = acc[i][n][2 * e + 1];
        hv[e] = g * u * __builtin_amdgcn_rcpf(1.0f + __expf(-g));
      }
      r[n] = cvt_pk(hv[0], hv[1]);
    }
    {
      auto t0 = __builtin_amdgcn_permlane32_swap(r[0], r[2], false, false); r[0] = t0[0]; r[2] = t0[1];
      auto t1 = __builtin_amdgcn_permlane32_swap(r[1], r[3], false, false); r[1] = t1[0]; r[3] = t1[1];
      auto t2 = __builtin_amdgcn_permlane16_swap(r[0], r[1], false, false); r[0] = t2[0]; r[1] = t2[1];
      auto t3 = __builtin_amdgcn_permlane16_swap(r[2], r[3], false, false); r[2] = t3[0]; r[3] = t3[1];
    }
    st_sc1(rsh, (((rb * 4 + rr) * NFT + ft) * 16 + sub) * 16, make_uint4(r[0], r[1], r[2], r[3]));
  }
}

#define FFN2_HPD 2
template <int NS>
__device__ __forceinline__ void ffn_phase_b(const uint4 (&wf)[6][4], __amdgpu_buffer_rsrc_t rsh, int hoff,
                                            f32x4 (&acc)[4][4]) {
  uint4 hf[FFN2_HPD][4];
#pragma unroll
  for (int s = 0; s < FFN2_HPD; ++s)
#pragma unroll
    for (int i = 0; i < 4; ++i) hf[s][i] = ld_sc1(rsh, hoff + i * (NFT * 256) + s * 1024);
  __builtin_amdgcn_sched_barrier(0);
#pragma unroll
  for (int s = 0; s < NS; ++s) {
#pragma unroll
    for (int m = 0; m < 4; ++m)
#pragma unroll
      for (int n = 0; n < 4; ++n)
        acc[m][n] = __builtin_amdgcn_mfma_f32_16x16x32_bf16(as_frag(wf[s][m]), as_frag(hf[s % FFN2_HPD][n]), acc[m][n], 0, 0, 0);
    __builtin_amdgcn_sched_barrier(0);
    if (s + FFN2_HPD < NS) {
#pragma unroll
      for (int i = 0; i < 4; ++i) hf[s % FFN2_HPD][i] = ld_sc1(rsh, hoff + i * (NFT * 256) + (s + FFN2_HPD) * 1024);
    }
    __builtin_amdgcn_sched_barrier(0);
  }
}

template <int LAYER>
__device__ __forceinline__ void ffn_layer(const u16* q, const u16* y, const u16* Wt, const u16* Wdt, u16* hT,
                                          u16* Xb, u16* out, gu32* cnt, unsigned char* lds, int rb, int fg) {
  asm volatile("" : "+s"(y), "+s"(Wt), "+s"(Wdt));
  const int tile0 = (fg < 4) ? 22 * fg : 88 + 21 * (fg - 4);
  const int ntiles = (fg < 4) ? 22 : 21;
  const int tid = threadIdx.x;
  const int w = __builtin_amdgcn_readfirstlane(tid >> 6), l = tid & 63;
  const int rr = l >> 4, sub = l & 15;
  const __amdgpu_buffer_rsrc_t rsx = __builtin_amdgcn_make_buffer_rsrc((void*)Xb, 0, S_LEN * D_DIM * 2, RSRC_W3);
  const __amdgpu_buffer_rsrc_t rsh =
      __builtin_amdgcn_make_buffer_rsrc((void*)(hT + (size_t)LAYER * (128 * NFT * 128)), 0, 128 * NFT * 256, RSRC_W3);
  uint4 yr[2][4];
#pragma unroll
  for (int p = 0; p < 2; ++p) {
    const int rowoff = (rb * 64 + w * 8 + p * 4 + rr) * D_DIM + sub * 8;
#pragma unroll
    for (int c = 0; c < 4; ++c) yr[p][c] = *(const uint4*)(y + rowoff + c * 128);
  }
  __builtin_amdgcn_sched_barrier(0);
  if (LAYER == 1) wait_for(cnt + 1, 8);
  if (w + 16 < ntiles) ffn_phase_a<3, LAYER == 1>(q, rsx, yr, Wt, rsh, lds, rb, fg, tile0);
  else ffn_phase_a<2, LAYER == 1>(q, rsx, yr, Wt, rsh, lds, rb, fg, tile0);
  STAMP(2, 5);
  publish(cnt + LAYER * 2);
  const int ks0 = w * 5 + min(w, 3);
  uint4 wf[6][4];
  {
    const uint4* wp = (const uint4*)Wdt + (size_t)(fg * 4) * (NKS2 * 64) + ks0 * 64 + l;
#pragma unroll
    for (int s = 0; s < 6; ++s)
#pragma unroll
      for (int i = 0; i < 4; ++i)
        wf[s][i] = wp[(size_t)i * (NKS2 * 64) + min(s, (w < 3) ? 5 : 4) * 64];
  }
  __builtin_amdgcn_sched_barrier(0);
  wait_for(cnt + LAYER * 2, 8);
  STAMP(2, 6);
  f32x4 acc[4][4];
#pragma unroll
  for (int m = 0; m < 4; ++m)
#pragma unroll
    for (int n = 0; n < 4; ++n) acc[m][n] = (f32x4){0.f, 0.f, 0.f, 0.f};
  const int hoff = (rb * 4) * (NFT * 256) + ks0 * 1024 + l * 16;
  if (w < 3) ffn_phase_b<6>(wf, rsh, hoff, acc);
  else ffn_phase_b<5>(wf, rsh, hoff, acc);
  STAMP(2, 7);
  float* red = (float*)lds;
#pragma unroll
  for (int m = 0; m < 4; ++m)
#pragma unroll
    for (int n = 0; n < 4; ++n)
      *(f32x4*)(red + (w * 64 + n * 16 + sub) * 68 + m * 16 + 4 * rr) = acc[m][n];
  __syncthreads();
  {
    const int tok = tid >> 3, no = tid & 7;
    f32x4 s0 = *(const f32x4*)(red + tok * 68 + no * 8), s1 = *(const f32x4*)(red + tok * 68 + no * 8 + 4);
#pragma unroll
    for (int ww = 1; ww < 8; ++ww) {
      s0 += *(const f32x4*)(red + (ww * 64 + tok) * 68 + no * 8);
      s1 += *(const f32x4*)(red + (ww * 64 + tok) * 68 + no * 8 + 4);
    }
    const uint4 rx = *(const uint4*)(lds + XRES_OFF + tok * 144 + no * 16);
    uint4 o;
    o.x = cvt_pk(lo_f(rx.x) + rbf(s0[0]), hi_f(rx.x) + rbf(s0[1]));
    o.y = cvt_pk(lo_f(rx.y) + rbf(s0[2]), hi_f(rx.y) + rbf(s0[3]));
    o.z = cvt_pk(lo_f(rx.z) + rbf(s1[0]), hi_f(rx.z) + rbf(s1[1]));
    o.w = cvt_pk(lo_f(rx.w) + rbf(s1[2]), hi_f(rx.w) + rbf(s1[3]));
    const int ooff = (rb * 64 + tok) * D_DIM + fg * 64 + no * 8;
    if (LAYER == 0) {
      st_sc1(rsx, ooff * 2, o);
      publish(cnt + 1);
    } else {
      *(uint4*)(out + ooff) = o;
    }
  }
}

__global__ __launch_bounds__(512, 2) void k_ffn(const u16* __restrict__ q, const u16* __restrict__ y,
                                                const u16* __restrict__ Wt, const u16* __restrict__ Wdt,
                                                u16* __restrict__ hT, u16* __restrict__ Xb,
                                                u16* __restrict__ out, unsigned* __restrict__ cnt_raw) {
  __shared__ __attribute__((aligned(16))) unsigned char lds[FFN_LDS];
  const int b = blockIdx.x;
  const int rb = (b & 7) * 4 + ((b >> 3) & 3);
  const int fg = b >> 5;
  gu32* cnt = (gu32*)cnt_raw + rb * 32;
  STAMP(2, 0);
  ffn_layer<0>(q, y, Wt, Wdt, hT, Xb, out, cnt, lds, rb, fg);
  STAMP(3, 0);
  ffn_layer<1>(q, y, Wt, Wdt, hT, Xb, out, cnt, lds, rb, fg);
  STAMP(3, 1);
}

extern "C" void kernel_launch(void* const* d_in, const int* in_sizes, int n_in, void* d_out, int out_size,
                              void* d_ws, size_t ws_size, hipStream_t stream) {
  const u16* q = (const u16*)d_in[0];
  const u16* k = (const u16*)d_in[1];
  const u16* v = (const u16*)d_in[2];
  const u16* shift_k = (const u16*)d_in[3];
  const u16* ssm_k = (const u16*)d_in[4];
  const u16* ssm_D = (const u16*)d_in[5];
  const u16* wg = (const u16*)d_in[7];
  const u16* wu = (const u16*)d_in[8];
  const u16* wd = (const u16*)d_in[9];
  u16* out = (u16*)d_out;

  unsigned char* ws = (unsigned char*)d_ws;
  size_t off = 0;
  u16* G = (u16*)(ws + off);   off += (size_t)D_DIM * G_STRIDE * 2;
  u16* Y = (u16*)(ws + off);   off += (size_t)S_LEN * D_DIM * 2;
  u16* Wt = (u16*)(ws + off);  off += (size_t)NFT * NKS1 * 1024;
  u16* Wdt = (u16*)(ws + off); off += (size_t)32 * NKS2 * 1024;
  u16* hT = (u16*)(ws + off);  off += (size_t)2 * 128 * NFT * 256;
  u16* Xb = (u16*)(ws + off);  off += (size_t)S_LEN * D_DIM * 2;
  unsigned* cnt = (unsigned*)(ws + off); off += 32 * 128;

  k_prep<<<512, 256, 0, stream>>>(k, v, shift_k, G, cnt);
  k_conv<<<256, 512, 0, stream>>>(G, ssm_k, ssm_D, Y, wg, wu, wd, Wt, Wdt);
  k_ffn<<<256, 512, 0, stream>>>(q, Y, Wt, Wdt, hT, Xb, out, cnt);
}
```

```cpp
#include <hip/hip_runtime.h>
#include <hip/hip_bf16.h>
#include <stdint.h>

typedef __attribute__((ext_vector_type(8))) short bf16x8;
typedef __attribute__((ext_vector_type(4))) float f32x4;
typedef unsigned short u16;
typedef unsigned int u32;

#define STAMP(k, i)

#define S_LEN 2048
#define D_DIM 512
#define F_DIM 1376
#define NFT 172
#define NKS1 16
#define NKS2 43
#define G_STRIDE 2112

__device__ __forceinline__ float bf2f(u16 v) { return __uint_as_float(((u32)v) << 16); }
__device__ __forceinline__ u16 f2bf(float f) {
  __bf16 b = (__bf16)f;
  return __builtin_bit_cast(u16, b);
}
__device__ __forceinline__ float rbf(float f) { return bf2f(f2bf(f)); }
__device__ __forceinline__ float lo_f(u32 w) { return __uint_as_float(w << 16); }
__device__ __forceinline__ float hi_f(u32 w) { return __uint_as_float(w & 0xffff0000u); }
__device__ __forceinline__ u32 pack2(float a, float b) { return (u32)f2bf(a) | ((u32)f2bf(b) << 16); }

__device__ __forceinline__ bf16x8 as_frag(uint4 v) { return __builtin_bit_cast(bf16x8, v); }

__global__ __launch_bounds__(256) void k_prep(const u16* __restrict__ kin, const u16* __restrict__ vin,
                                              const u16* __restrict__ shift_k, u16* __restrict__ G,
                                              unsigned* __restrict__ cnt) {
  __shared__ __attribute__((aligned(16))) u16 T[64 * 34];
  const int tid = threadIdx.x;
  const int b = blockIdx.x;
  STAMP(0, 0);
  if (b == 0) {
#pragma unroll
    for (int i = 0; i < 4; ++i) cnt[tid + i * 256] = 0u;
  }
  const int tb = b >> 3, db = b & 7;
  const int t0 = tb * 32, d0 = db * 64;
  const int oct = tid & 7, r = tid >> 3;
  const int t = t0 + r;
  uint4 kk[4], vv, sk[4];
#pragma unroll
  for (int i = 0; i < 4; ++i) {
    int tt = t - 3 + i;
    kk[i] = (tt >= 0) ? *(const uint4*)(kin + (size_t)tt * D_DIM + d0 + oct * 8) : make_uint4(0, 0, 0, 0);
  }
  vv = *(const uint4*)(vin + (size_t)t * D_DIM + d0 + oct * 8);
#pragma unroll
  for (int i = 0; i < 4; ++i) sk[i] = *(const uint4*)(shift_k + (size_t)(d0 + oct * 8) * 4 + i * 8);
  const u32* kw = (const u32*)kk;
  const u32* vw = (const u32*)&vv;
  const u32* sw = (const u32*)sk;
#pragma unroll
  for (int c = 0; c < 8; ++c) {
    float w[4];
    w[0] = lo_f(sw[c * 2]); w[1] = hi_f(sw[c * 2]); w[2] = lo_f(sw[c * 2 + 1]); w[3] = hi_f(sw[c * 2 + 1]);
    float acc = 0.f;
#pragma unroll
    for (int tp = 0; tp < 4; ++tp) {
      u32 kwv = kw[tp * 4 + (c >> 1)];
      float kval = (c & 1) ? hi_f(kwv) : lo_f(kwv);
      float prod = rbf(kval * w[3 - tp]);
      acc = rbf(acc + prod);
    }
    float vval = (c & 1) ? hi_f(vw[c >> 1]) : lo_f(vw[c >> 1]);
    T[(oct * 8 + c) * 34 + r] = f2bf(acc * vval);
  }
  __syncthreads();
  const int hw = tid >> 5, l32 = tid & 31;
#pragma unroll
  for (int c = 0; c < 8; ++c) {
    int dd = hw * 8 + c;
    G[(size_t)(d0 + dd) * G_STRIDE + (2079 - (t0 + l32))] = T[dd * 34 + l32];
  }
  if (tb == 0) {
#pragma unroll
    for (int c = 0; c < 8; ++c) G[(size_t)(d0 + hw * 8 + c) * G_STRIDE + 2080 + l32] = 0;
  }
  if (tb == 63) {
#pragma unroll
    for (int c = 0; c < 8; ++c) G[(size_t)(d0 + hw * 8 + c) * G_STRIDE + l32] = 0;
  }
  STAMP(0, 1);
}

#define KC_BYTES 4416
#define HI_BYTES 5408
#define CH_BYTES (8 * KC_BYTES + 4 * HI_BYTES)
#define RT_OFF (2 * CH_BYTES)
#define CONV_LDS (RT_OFF + 2 * 3 * 32 * 72 * 2)
#define W1_UNITS (16 * 22)
#define W2_UNITS (43 * 8)

#define CONV_PD 4
__device__ __forceinline__ constexpr int conv_nt(int s) { return 4 - (s >= 17) - (s >= 33) - (s >= 49); }

template <int S0, int S1>
__device__ __forceinline__ void conv_range(const unsigned char* kcb, const unsigned char* hib, f32x4 (&acc)[4][2]) {
  uint4 bb[CONV_PD][2], aa[CONV_PD][4];
#pragma unroll
  for (int p = 0; p < CONV_PD - 1; ++p) {
    const int s = S0 + p;
    if (s < S1) {
#pragma unroll
      for (int nt = 0; nt < 2; ++nt) bb[p][nt] = *(const uint4*)(kcb - (16 * nt + 32 * s) * 2);
#pragma unroll
      for (int a = 0; a < 4; ++a)
        if (a < conv_nt(s)) aa[p][a] = *(const uint4*)(hib + (512 * (3 - a) - 32 * s) * 2);
    }
  }
  __builtin_amdgcn_sched_barrier(0);
#pragma unroll
  for (int s = S0; s < S1; ++s) {
    const int sn = s + CONV_PD - 1;
    if (sn < S1) {
      const int p = (sn - S0) % CONV_PD;
#pragma unroll
      for (int nt = 0; nt < 2; ++nt) bb[p][nt] = *(const uint4*)(kcb - (16 * nt + 32 * sn) * 2);
#pragma unroll
      for (int a = 0; a < 4; ++a)
        if (a < conv_nt(sn)) aa[p][a] = *(const uint4*)(hib + (512 * (3 - a) - 32 * sn) * 2);
    }
    __builtin_amdgcn_sched_barrier(0);
    const int p = (s - S0) % CONV_PD;
#pragma unroll
    for (int a = 0; a < 4; ++a)
      if (a < conv_nt(s)) {
#pragma unroll
        for (int nt = 0; nt < 2; ++nt)
          acc[3 - a][nt] = __builtin_amdgcn_mfma_f32_16x16x32_bf16(as_frag(bb[p][nt]), as_frag(aa[p][a]), acc[3 - a][nt], 0, 0, 0);
      }
    __builtin_amdgcn_sched_barrier(0);
  }
}

__global__ __launch_bounds__(512) void k_conv(const u16* __restrict__ G, const u16* __restrict__ ssm_k,
                                              const u16* __restrict__ ssm_D, u16* __restrict__ y,
                                              const u16* __restrict__ wg, const u16* __restrict__ wu,
                                              const u16* __restrict__ wd, u16* __restrict__ Wt,
                                              u16* __restrict__ Wdt) {
  __shared__ __attribute__((aligned(16))) unsigned char smem[CONV_LDS];
  const int tid = threadIdx.x;
  const int ch = tid >> 8, t256 = tid & 255;
  const int pb = (blockIdx.x & 7) * 32 + (blockIdx.x >> 3);
  const int d = pb * 2 + ch;
  unsigned char* base = smem + ch * CH_BYTES;
  unsigned char* kc = base;
  unsigned char* hi = base + 8 * KC_BYTES;

  STAMP(1, 0);
  const uint4* gsrc = (const uint4*)(G + (size_t)d * G_STRIDE);
  const uint4* hsrc = (const uint4*)(ssm_k + (size_t)d * S_LEN);
  uint4 g0 = gsrc[t256], g1 = gsrc[t256 + 1];
  uint4 g2 = make_uint4(0, 0, 0, 0), g3 = make_uint4(0, 0, 0, 0);
  if (t256 < 7) { g2 = gsrc[256 + t256]; g3 = gsrc[257 + t256]; }
  uint4 h0 = hsrc[t256];

  const int rrow = t256 >> 3, roct = t256 & 7;
  uint4 wl[2][2];
#pragma unroll
  for (int rnd = 0; rnd < 2; ++rnd) {
    const int u = blockIdx.x + rnd * 256;
    wl[rnd][0] = make_uint4(0, 0, 0, 0); wl[rnd][1] = make_uint4(0, 0, 0, 0);
    if (ch == 0) {
      const int ks = u / 22, fb = u % 22;
      const int f = fb * 64 + roct * 8;
      if (u < W1_UNITS && f < F_DIM) {
        wl[rnd][0] = *(const uint4*)(wg + (size_t)(ks * 32 + rrow) * F_DIM + f);
        wl[rnd][1] = *(const uint4*)(wu + (size_t)(ks * 32 + rrow) * F_DIM + f);
      }
    } else {
      if (u < W2_UNITS) {
        const int ks = u >> 3, nb = u & 7;
        wl[rnd][0] = *(const uint4*)(wd + (size_t)(ks * 32 + rrow) * D_DIM + nb * 64 + roct * 8);
      }
    }
  }

#pragma unroll
  for (int rep = 0; rep < 2; ++rep) {
    const int q = t256 + rep * 256;
    if (rep == 0 || t256 < 7) {
      uint4 lo = rep ? g2 : g0, hh = rep ? g3 : g1;
      u32 W[8] = {lo.x, lo.y, lo.z, lo.w, hh.x, hh.y, hh.z, hh.w};
#pragma unroll
      for (int e = 0; e < 8; ++e) {
        const int sft = 7 - e;
        u32 o[4];
#pragma unroll
        for (int j = 0; j < 4; ++j) {
          if (sft & 1) o[j] = __builtin_amdgcn_alignbit(W[(sft >> 1) + j + 1], W[(sft >> 1) + j], 16);
          else o[j] = W[(sft >> 1) + j];
        }
        *(uint4*)(kc + e * KC_BYTES + q * 16) = make_uint4(o[0], o[1], o[2], o[3]);
      }
    }
  }
  {
#pragma unroll
    for (int c = 0; c < 4; ++c) *(uint4*)(hi + c * HI_BYTES + (64 + t256) * 16) = h0;
    if (t256 < 68) {
      const int q = (t256 < 64) ? t256 : (256 + t256);
#pragma unroll
      for (int c = 0; c < 4; ++c) *(uint4*)(hi + c * HI_BYTES + q * 16) = make_uint4(0, 0, 0, 0);
    }
  }
  STAMP(1, 1);
  __syncthreads();
  STAMP(1, 2);
  const int l = tid & 63, wq = (tid >> 6) & 3;
  const int c16 = l & 15, hh = l >> 4;
  const unsigned char* kcb = kc + (c16 & 7) * KC_BYTES + (2072 + 8 * hh - (c16 & 8)) * 2;
  const unsigned char* hib = hi + (c16 >> 2) * HI_BYTES + (512 + 32 * c16 + 8 * hh) * 2;
  f32x4 acc[4][2];
#pragma unroll
  for (int a = 0; a < 4; ++a)
#pragma unroll
    for (int nt = 0; nt < 2; ++nt) acc[a][nt] = (f32x4){0.f, 0.f, 0.f, 0.f};

  {
    const int wqu = __builtin_amdgcn_readfirstlane(wq);
    if (wqu == 0) conv_range<0, 10>(kcb, hib, acc);
    else if (wqu == 1) conv_range<10, 21>(kcb, hib, acc);
    else if (wqu == 2) conv_range<21, 36>(kcb, hib, acc);
    else conv_range<36, 65>(kcb, hib, acc);
  }

  u16* sc = (u16*)(smem + RT_OFF);
#pragma unroll
  for (int rnd = 0; rnd < 2; ++rnd) {
    u16* scr = sc + rnd * (3 * 32 * 72);
    if (ch == 0) {
      *(uint4*)(scr + rrow * 72 + roct * 8) = wl[rnd][0];
      *(uint4*)(scr + 32 * 72 + rrow * 72 + roct * 8) = wl[rnd][1];
    } else {
      *(uint4*)(scr + 64 * 72 + rrow * 72 + roct * 8) = wl[rnd][0];
    }
  }
  STAMP(1, 3);
  __syncthreads();
  STAMP(1, 4);
  float* red = (float*)(base + KC_BYTES);
#pragma unroll
  for (int a = 0; a < 4; ++a)
#pragma unroll
    for (int nt = 0; nt < 2; ++nt)
      *(f32x4*)(red + wq * 2304 + (16 * a + c16) * 36 + nt * 16 + 4 * hh) = acc[a][nt];
  __syncthreads();
#pragma unroll
  for (int rnd = 0; rnd < 2; ++rnd) {
    const int u = blockIdx.x + rnd * 256;
    const u16* scr = sc + rnd * (3 * 32 * 72);
    if (ch == 0) {
      if (u < W1_UNITS) {
        const int ks = u / 22, fb = u % 22;
#pragma unroll
        for (int rep = 0; rep < 2; ++rep) {
          int it = t256 + rep * 256;
          int tl = it >> 6, l = it & 63;
          int tile = fb * 8 + tl;
          if (tile < NFT) {
            int rr = l & 15;
            int fl = tl * 8 + (rr >> 1);
            const u16* src = scr + (rr & 1) * 32 * 72;
            int kq = (l >> 4) * 8;
            u32 o[4];
#pragma unroll
            for (int j = 0; j < 4; ++j)
              o[j] = (u32)src[(kq + 2 * j) * 72 + fl] | ((u32)src[(kq + 2 * j + 1) * 72 + fl] << 16);
            *(uint4*)(Wt + ((size_t)(tile * NKS1 + ks) * 64 + l) * 8) = make_uint4(o[0], o[1], o[2], o[3]);
          }
        }
      }
    } else {
      if (u < W2_UNITS) {
        const int ks = u >> 3, nb = u & 7;
        const u16* src = scr + 64 * 72;
        int nt = t256 >> 6, l = t256 & 63;
        int n = nt * 16 + (l & 15);
        int kq = (l >> 4) * 8;
        u32 o[4];
#pragma unroll
        for (int j = 0; j < 4; ++j)
          o[j] = (u32)src[(kq + 2 * j) * 72 + n] | ((u32)src[(kq + 2 * j + 1) * 72 + n] << 16);
        *(uint4*)(Wdt + ((size_t)((nb * 4 + nt) * NKS2 + ks) * 64 + l) * 8) = make_uint4(o[0], o[1], o[2], o[3]);
      }
    }
  }
  {
    const int t = tid * 4;
    const int ro = (t >> 5) * 36 + (t & 31);
    float res[2][4];
#pragma unroll
    for (int cc = 0; cc < 2; ++cc) {
      const unsigned char* bb = smem + cc * CH_BYTES;
      const float* rd = (const float*)(bb + KC_BYTES);
      f32x4 s = *(const f32x4*)(rd + ro);
#pragma unroll
      for (int w = 1; w < 4; ++w) s += *(const f32x4*)(rd + w * 2304 + ro);
      float Dv = bf2f(ssm_D[pb * 2 + cc]);
      const u16* k0 = (const u16*)bb;
#pragma unroll
      for (int r = 0; r < 4; ++r) {
        float kvv = bf2f(k0[2072 - (t + r)]);
        res[cc][r] = rbf(s[r]) + rbf(kvv * Dv);
      }
    }
#pragma unroll
    for (int r = 0; r < 4; ++r)
      *(u32*)(y + (size_t)(t + r) * D_DIM + pb * 2) = pack2(res[0][r], res[1][r]);
  }
  STAMP(1, 5);
}

template <int CTRL>
__device__ __forceinline__ float dpp_add(float x) {
  int v = __builtin_amdgcn_update_dpp(0, __float_as_int(x), CTRL, 0xf, 0xf, true);
  return x + __int_as_float(v);
}
__device__ __forceinline__ float row16_sum(float x) {
  x = dpp_add<0xB1>(x);
  x = dpp_add<0x4E>(x);
  x = dpp_add<0x141>(x);
  x = dpp_add<0x140>(x);
  return x;
}

typedef __attribute__((ext_vector_type(2))) float f32x2;
typedef __attribute__((ext_vector_type(2))) __bf16 bf16x2_t;
__device__ __forceinline__ u32 cvt_pk(float a, float b) {
  f32x2 v = {a, b};
  bf16x2_t r = __builtin_convertvector(v, bf16x2_t);
  return __builtin_bit_cast(u32, r);
}
__device__ __forceinline__ void ln_stats(const u32 (&xp)[16], float& inv, float& nmi) {
  const bf16x2_t one = __builtin_bit_cast(bf16x2_t, 0x3f803f80u);
  float s0 = 0.f, s1 = 0.f, q0 = 0.f, q1 = 0.f;
#pragma unroll
  for (int i = 0; i < 16; i += 2) {
    bf16x2_t a = __builtin_bit_cast(bf16x2_t, xp[i]), b = __builtin_bit_cast(bf16x2_t, xp[i + 1]);
    s0 = __builtin_amdgcn_fdot2_f32_bf16(a, one, s0, false);
    q0 = __builtin_amdgcn_fdot2_f32_bf16(a, a, q0, false);
    s1 = __builtin_amdgcn_fdot2_f32_bf16(b, one, s1, false);
    q1 = __builtin_amdgcn_fdot2_f32_bf16(b, b, q1, false);
  }
  float mean = row16_sum(s0 + s1) * (1.0f / 512.0f);
  float ex2 = row16_sum(q0 + q1) * (1.0f / 512.0f);
  float var = rbf(fmaxf(ex2 - mean * mean, 0.f));
  float den = rbf(__builtin_amdgcn_sqrtf(rbf(var + 1e-5f)));
  inv = __builtin_amdgcn_rcpf(den);
  nmi = -rbf(mean) * inv;
}

#define FFN1_PD 6
#define AUX_SC1 16
#define RSRC_W3 0x00020000
typedef unsigned v4u __attribute__((ext_vector_type(4)));
typedef __attribute__((address_space(1))) unsigned gu32;
#define RLX_AGENT __ATOMIC_RELAXED, __HIP_MEMORY_SCOPE_AGENT
#define SPIN_CAP (1u << 20)

__device__ __forceinline__ uint4 ld_sc1(__amdgpu_buffer_rsrc_t rs, int off) {
  v4u v = __builtin_amdgcn_raw_buffer_load_b128(rs, off, 0, AUX_SC1);
  return make_uint4(v.x, v.y, v.z, v.w);
}
__device__ __forceinline__ void st_sc1(__amdgpu_buffer_rsrc_t rs, int off, uint4 v) {
  v4u t = {v.x, v.y, v.z, v.w};
  __builtin_amdgcn_raw_buffer_store_b128(t, rs, off, 0, AUX_SC1);
}
__device__ __forceinline__ void publish(gu32* cnt) {
  asm volatile("s_waitcnt vmcnt(0)" ::: "memory");
  __syncthreads();
  if (threadIdx.x == 0) __hip_atomic_fetch_add(cnt, 1u, RLX_AGENT);
}
__device__ __forceinline__ void wait_for(gu32* cnt, unsigned want) {
  if (threadIdx.x == 0) {
    for (unsigned spins = 0; spins < SPIN_CAP; ++spins) {
      if (__hip_atomic_load(cnt, RLX_AGENT) >= want) break;
      __builtin_amdgcn_s_sleep(1);
    }
    __builtin_amdgcn_fence(__ATOMIC_ACQUIRE, "wavefront");
  }
  __syncthreads();
}

#define XS_BYTES 65536
#define RED_BYTES (8 * 64 * 68 * 4)
#define XRES_OFF RED_BYTES
#define FFN_LDS (RED_BYTES + 64 * 144)

template <int NT, bool XSC1>
__device__ __forceinline__ void ffn_phase_a(const u16* __restrict__ xin, __amdgpu_buffer_rsrc_t rsx,
                                            const uint4 (&yr)[2][4], const u16* __restrict__ Wt,
                                            __amdgpu_buffer_rsrc_t rsh, unsigned char* lds, int rb, int fg, int tile0,
                                            gu32* xcnt) {
  const int tid = threadIdx.x;
  const int w = tid >> 6, l = tid & 63;
  const int rr = l >> 4, sub = l & 15;
  unsigned char* xs = lds;

  const uint4* wbase[NT];
#pragma unroll
  for (int i = 0; i < NT; ++i) wbase[i] = (const uint4*)(Wt) + (size_t)(tile0 + w + 8 * i) * (NKS1 * 64) + l;
  uint4 wf[FFN1_PD][NT];
  uint4 xr[2][4];
  if (XSC1) wait_for(xcnt, 8);
#pragma unroll
  for (int p = 0; p < 2; ++p) {
    const int rowoff = (rb * 64 + w * 8 + p * 4 + rr) * D_DIM + sub * 8;
#pragma unroll
    for (int c = 0; c < 4; ++c) {
      if (XSC1) xr[p][c] = ld_sc1(rsx, (rowoff + c * 128) * 2);
      else xr[p][c] = *(const uint4*)(xin + rowoff + c * 128);
    }
  }
  __builtin_amdgcn_sched_barrier(0);
#pragma unroll
  for (int s = 0; s < 2; ++s)
#pragma unroll
    for (int i = 0; i < NT; ++i) wf[s][i] = wbase[i][s * 64];
  __builtin_amdgcn_sched_barrier(0);
#pragma unroll
  for (int p = 0; p < 2; ++p) {
    const int row = w * 8 + p * 4 + rr;
    u32 xp[16];
#pragma unroll
    for (int c = 0; c < 4; ++c) { xp[c * 4] = xr[p][c].x; xp[c * 4 + 1] = xr[p][c].y; xp[c * 4 + 2] = xr[p][c].z; xp[c * 4 + 3] = xr[p][c].w; }
    float inv, nmi;
    ln_stats(xp, inv, nmi);
    f32x2 inv2 = {inv, inv}, nmi2 = {nmi, nmi};
    f32x2 rv[16];
#pragma unroll
    for (int c = 0; c < 4; ++c) {
      const u32* yw = (const u32*)&yr[p][c];
#pragma unroll
      for (int j = 0; j < 4; ++j) {
        f32x2 xv = {lo_f(xp[c * 4 + j]), hi_f(xp[c * 4 + j])};
        f32x2 yv = {lo_f(yw[j]), hi_f(yw[j])};
        f32x2 ln = xv * inv2 + nmi2;
        rv[c * 4 + j] = ln * yv + xv;
        xp[c * 4 + j] = cvt_pk(rv[c * 4 + j].x, rv[c * 4 + j].y);
      }
    }
    {
      const int cc = fg >> 1, s0 = (fg & 1) * 8;
      if (sub >= s0 && sub < s0 + 8) {
        uint4 o = make_uint4(0, 0, 0, 0);
#pragma unroll
        for (int c = 0; c < 4; ++c)
          if (c == cc) o = make_uint4(xp[c * 4], xp[c * 4 + 1], xp[c * 4 + 2], xp[c * 4 + 3]);
        *(uint4*)(lds + XRES_OFF + row * 144 + (sub - s0) * 16) = o;
      }
    }
    ln_stats(xp, inv, nmi);
    inv2 = (f32x2){inv, inv}; nmi2 = (f32x2){nmi, nmi};
    const int ntile = row >> 4, tok = row & 15;
#pragma unroll
    for (int c = 0; c < 4; ++c) {
      u32 o[4];
#pragma unroll
      for (int j = 0; j < 4; ++j) { f32x2 r = rv[c * 4 + j] * inv2 + nmi2; o[j] = cvt_pk(r.x, r.y); }
      const int kstep = c * 4 + (sub >> 2), chunk = sub & 3;
      *(uint4*)(xs + ((ntile * 16 + kstep) * 64 + chunk * 16 + (tok ^ sub)) * 16) = make_uint4(o[0], o[1], o[2], o[3]);
    }
    __builtin_amdgcn_sched_barrier(0);
#pragma unroll
    for (int s = 2 + 2 * p; s < 4 + 2 * p; ++s)
#pragma unroll
      for (int i = 0; i < NT; ++i) wf[s][i] = wbase[i][s * 64];
    __builtin_amdgcn_sched_barrier(0);
  }
  STAMP(2, 2);
  __syncthreads();
  STAMP(2, 3);
  f32x4 acc[NT][4];
#pragma unroll
  for (int i = 0; i < NT; ++i)
#pragma unroll
    for (int n = 0; n < 4; ++n) acc[i][n] = (f32x4){0.f, 0.f, 0.f, 0.f};
  uint4 xf[2][4];
#pragma unroll
  for (int n = 0; n < 4; ++n) xf[0][n] = *(const uint4*)(xs + ((n * 16) * 64 + rr * 16 + (sub ^ rr)) * 16);
  __builtin_amdgcn_sched_barrier(0);
#pragma unroll
  for (int s = 0; s < NKS1; ++s) {
    if (s + 1 < NKS1) {
      const int swz = sub ^ (((s + 1) * 4 + rr) & 15);
#pragma unroll
      for (int n = 0; n < 4; ++n) xf[(s + 1) & 1][n] = *(const uint4*)(xs + ((n * 16 + s + 1) * 64 + rr * 16 + swz) * 16);
    }
    __builtin_amdgcn_sched_barrier(0);
#pragma unroll
    for (int i = 0; i < NT; ++i)
#pragma unroll
      for (int n = 0; n < 4; ++n)
        acc[i][n] = __builtin_amdgcn_mfma_f32_16x16x32_bf16(as_frag(wf[s % FFN1_PD][i]), as_frag(xf[s & 1][n]), acc[i][n], 0, 0, 0);
    __builtin_amdgcn_sched_barrier(0);
    if (s + FFN1_PD < NKS1) {
#pragma unroll
      for (int i = 0; i < NT; ++i) wf[s % FFN1_PD][i] = wbase[i][(s + FFN1_PD) * 64];
    }
    __builtin_amdgcn_sched_barrier(0);
  }
  STAMP(2, 4);
#pragma unroll
  for (int i = 0; i < NT; ++i) {
    const int ft = tile0 + w + 8 * i;
    u32 r[4];
#pragma unroll
    for (int n = 0; n < 4; ++n) {
      float hv[2];
#pragma unroll
      for (int e = 0; e < 2; ++e) {
        float g = rbf(acc[i][n][2 * e]);
        float u = rbf(acc[i][n][2 * e + 1]);
        hv[e] = rbf(g * __builtin_amdgcn_rcpf(1.0f + __expf(-g))) * u;
      }
      r[n] = cvt_pk(hv[0], hv[1]);
    }
    {
      auto t0 = __builtin_amdgcn_permlane32_swap(r[0], r[2], false, false); r[0] = t0[0]; r[2] = t0[1];
      auto t1 = __builtin_amdgcn_permlane32_swap(r[1], r[3], false, false); r[1] = t1[0]; r[3] = t1[1];
      auto t2 = __builtin_amdgcn_permlane16_swap(r[0], r[1], false, false); r[0] = t2[0]; r[1] = t2[1];
      auto t3 = __builtin_amdgcn_permlane16_swap(r[2], r[3], false, false); r[2] = t3[0]; r[3] = t3[1];
    }
    st_sc1(rsh, (((rb * 4 + rr) * NFT + ft) * 16 + sub) * 16, make_uint4(r[0], r[1], r[2], r[3]));
  }
}

#define FFN2_HPD 2
template <int NS>
__device__ __forceinline__ void ffn_phase_b(const uint4 (&wf)[6][4], __amdgpu_buffer_rsrc_t rsh, int hoff,
                                            f32x4 (&acc)[4][4]) {
  uint4 hf[FFN2_HPD][4];
#pragma unroll
  for (int s = 0; s < FFN2_HPD; ++s)
#pragma unroll
    for (int i = 0; i < 4; ++i) hf[s][i] = ld_sc1(rsh, hoff + i * (NFT * 256) + s * 1024);
  __builtin_amdgcn_sched_barrier(0);
#pragma unroll
  for (int s = 0; s < NS; ++s) {
#pragma unroll
    for (int m = 0; m < 4; ++m)
#pragma unroll
      for (int n = 0; n < 4; ++n)
        acc[m][n] = __builtin_amdgcn_mfma_f32_16x16x32_bf16(as_frag(wf[s][m]), as_frag(hf[s % FFN2_HPD][n]), acc[m][n], 0, 0, 0);
    __builtin_amdgcn_sched_barrier(0);
    if (s + FFN2_HPD < NS) {
#pragma unroll
      for (int i = 0; i < 4; ++i) hf[s % FFN2_HPD][i] = ld_sc1(rsh, hoff + i * (NFT * 256) + (s + FFN2_HPD) * 1024);
    }
    __builtin_amdgcn_sched_barrier(0);
  }
}

template <int LAYER>
__device__ __forceinline__ void ffn_layer(const u16* q, const u16* y, const u16* Wt, const u16* Wdt, u16* hT,
                                          u16* Xb, u16* out, gu32* cnt, unsigned char* lds, int rb, int fg) {
  asm volatile("" : "+s"(y), "+s"(Wt), "+s"(Wdt));
  const int tile0 = (fg < 4) ? 22 * fg : 88 + 21 * (fg - 4);
  const int ntiles = (fg < 4) ? 22 : 21;
  const int tid = threadIdx.x;
  const int w = __builtin_amdgcn_readfirstlane(tid >> 6), l = tid & 63;
  const int rr = l >> 4, sub = l & 15;
  const __amdgpu_buffer_rsrc_t rsx = __builtin_amdgcn_make_buffer_rsrc((void*)Xb, 0, S_LEN * D_DIM * 2, RSRC_W3);
  const __amdgpu_buffer_rsrc_t rsh =
      __builtin_amdgcn_make_buffer_rsrc((void*)(hT + (size_t)LAYER * (128 * NFT * 128)), 0, 128 * NFT * 256, RSRC_W3);
  uint4 yr[2][4];
#pragma unroll
  for (int p = 0; p < 2; ++p) {
    const int rowoff = (rb * 64 + w * 8 + p * 4 + rr) * D_DIM + sub * 8;
#pragma unroll
    for (int c = 0; c < 4; ++c) yr[p][c] = *(const uint4*)(y + rowoff + c * 128);
  }
  __builtin_amdgcn_sched_barrier(0);
  if (w + 16 < ntiles) ffn_phase_a<3, LAYER == 1>(q, rsx, yr, Wt, rsh, lds, rb, fg, tile0, cnt + 1);
  else ffn_phase_a<2, LAYER == 1>(q, rsx, yr, Wt, rsh, lds, rb, fg, tile0, cnt + 1);
  STAMP(2, 5);
  publish(cnt + LAYER * 2);
  const int ks0 = w * 5 + min(w, 3);
  uint4 wf[6][4];
  {
    const uint4* wp = (const uint4*)Wdt + (size_t)(fg * 4) * (NKS2 * 64) + ks0 * 64 + l;
#pragma unroll
    for (int s = 0; s < 6; ++s)
#pragma unroll
      for (int i = 0; i < 4; ++i)
        wf[s][i] = wp[(size_t)i * (NKS2 * 64) + min(s, (w < 3) ? 5 : 4) * 64];
  }
  __builtin_amdgcn_sched_barrier(0);
  wait_for(cnt + LAYER * 2, 8);
  STAMP(2, 6);
  f32x4 acc[4][4];
#pragma unroll
  for (int m = 0; m < 4; ++m)
#pragma unroll
    for (int n = 0; n < 4; ++n) acc[m][n] = (f32x4){0.f, 0.f, 0.f, 0.f};
  const int hoff = (rb * 4) * (NFT * 256) + ks0 * 1024 + l * 16;
  if (w < 3) ffn_phase_b<6>(wf, rsh, hoff, acc);
  else ffn_phase_b<5>(wf, rsh, hoff, acc);
  STAMP(2, 7);
  float* red = (float*)lds;
#pragma unroll
  for (int m = 0; m < 4; ++m)
#pragma unroll
    for (int n = 0; n < 4; ++n)
      *(f32x4*)(red + (w * 64 + n * 16 + sub) * 68 + m * 16 + 4 * rr) = acc[m][n];
  __syncthreads();
  {
    const int tok = tid >> 3, no = tid & 7;
    f32x4 s0 = *(const f32x4*)(red + tok * 68 + no * 8), s1 = *(const f32x4*)(red + tok * 68 + no * 8 + 4);
#pragma unroll
    for (int ww = 1; ww < 8; ++ww) {
      s0 += *(const f32x4*)(red + (ww * 64 + tok) * 68 + no * 8);
      s1 += *(const f32x4*)(red + (ww * 64 + tok) * 68 + no * 8 + 4);
    }
    const uint4 rx = *(const uint4*)(lds + XRES_OFF + tok * 144 + no * 16);
    uint4 o;
    o.x = cvt_pk(lo_f(rx.x) + rbf(s0[0]), hi_f(rx.x) + rbf(s0[1]));
    o.y = cvt_pk(lo_f(rx.y) + rbf(s0[2]), hi_f(rx.y) + rbf(s0[3]));
    o.z = cvt_pk(lo_f(rx.z) + rbf(s1[0]), hi_f(rx.z) + rbf(s1[1]));
    o.w = cvt_pk(lo_f(rx.w) + rbf(s1[2]), hi_f(rx.w) + rbf(s1[3]));
    const int ooff = (rb * 64 + tok) * D_DIM + fg * 64 + no * 8;
    if (LAYER == 0) {
      st_sc1(rsx, ooff * 2, o);
      publish(cnt + 1);
    } else {
      *(uint4*)(out + ooff) = o;
    }
  }
}

__global__ __launch_bounds__(512, 2) void k_ffn(const u16* __restrict__ q, const u16* __restrict__ y,
                                                const u16* __restrict__ Wt, const u16* __restrict__ Wdt,
                                                u16* __restrict__ hT, u16* __restrict__ Xb,
                                                u16* __restrict__ out, unsigned* __restrict__ cnt_raw) {
  __shared__ __attribute__((aligned(16))) unsigned char lds[FFN_LDS];
  const int b = blockIdx.x;
  const int rb = (b & 7) * 4 + ((b >> 3) & 3);
  const int fg = b >> 5;
  gu32* cnt = (gu32*)cnt_raw + rb * 32;
  STAMP(2, 0);
  ffn_layer<0>(q, y, Wt, Wdt, hT, Xb, out, cnt, lds, rb, fg);
  STAMP(3, 0);
  ffn_layer<1>(q, y, Wt, Wdt, hT, Xb, out, cnt, lds, rb, fg);
  STAMP(3, 1);
}

extern "C" void kernel_launch(void* const* d_in, const int* in_sizes, int n_in, void* d_out, int out_size,
                              void* d_ws, size_t ws_size, hipStream_t stream) {
  const u16* q = (const u16*)d_in[0];
  const u16* k = (const u16*)d_in[1];
  const u16* v = (const u16*)d_in[2];
  const u16* shift_k = (const u16*)d_in[3];
  const u16* ssm_k = (const u16*)d_in[4];
  const u16* ssm_D = (const u16*)d_in[5];
  const u16* wg = (const u16*)d_in[7];
  const u16* wu = (const u16*)d_in[8];
  const u16* wd = (const u16*)d_in[9];
  u16* out = (u16*)d_out;

  unsigned char* ws = (unsigned char*)d_ws;
  size_t off = 0;
  u16* G = (u16*)(ws + off);   off += (size_t)D_DIM * G_STRIDE * 2;
  u16* Y = (u16*)(ws + off);   off += (size_t)S_LEN * D_DIM * 2;
  u16* Wt = (u16*)(ws + off);  off += (size_t)NFT * NKS1 * 1024;
  u16* Wdt = (u16*)(ws + off); off += (size_t)32 * NKS2 * 1024;
  u16* hT = (u16*)(ws + off);  off += (size_t)2 * 128 * NFT * 256;
  u16* Xb = (u16*)(ws + off);  off += (size_t)S_LEN * D_DIM * 2;
  unsigned* cnt = (unsigned*)(ws + off); off += 32 * 128;

  k_prep<<<512, 256, 0, stream>>>(k, v, shift_k, G, cnt);
  k_conv<<<256, 512, 0, stream>>>(G, ssm_k, ssm_D, Y, wg, wu, wd, Wt, Wdt);
  k_ffn<<<256, 512, 0, stream>>>(q, Y, Wt, Wdt, hT, Xb, out, cnt);
}
```

```cpp
#include <hip/hip_runtime.h>
#include <hip/hip_bf16.h>
#include <stdint.h>

typedef __attribute__((ext_vector_type(8))) short bf16x8;
typedef __attribute__((ext_vector_type(4))) float f32x4;
typedef unsigned short u16;
typedef unsigned int u32;


#define S_LEN 2048
#define D_DIM 512
#define F_DIM 1376
#define NFT 172
#define NKS1 16
#define NKS2 43
#define G_STRIDE 2112

__device__ __forceinline__ float bf2f(u16 v) { return __uint_as_float(((u32)v) << 16); }
__device__ __forceinline__ u16 f2bf(float f) {
  __bf16 b = (__bf16)f;
  return __builtin_bit_cast(u16, b);
}
__device__ __forceinline__ float rbf(float f) { return bf2f(f2bf(f)); }
__device__ __forceinline__ float lo_f(u32 w) { return __uint_as_float(w << 16); }
__device__ __forceinline__ float hi_f(u32 w) { return __uint_as_float(w & 0xffff0000u); }
__device__ __forceinline__ u32 pack2(float a, float b) { return (u32)f2bf(a) | ((u32)f2bf(b) << 16); }

__device__ __forceinline__ bf16x8 as_frag(uint4 v) { return __builtin_bit_cast(bf16x8, v); }

__global__ __launch_bounds__(256) void k_prep(const u16* __restrict__ kin, const u16* __restrict__ vin,
                                              const u16* __restrict__ shift_k, u16* __restrict__ G,
                                              unsigned* __restrict__ cnt) {
  __shared__ __attribute__((aligned(16))) u16 T[64 * 34];
  const int tid = threadIdx.x;
  const int b = blockIdx.x;
  if (b == 0) {
#pragma unroll
    for (int i = 0; i < 4; ++i) cnt[tid + i * 256] = 0u;
  }
  const int tb = b >> 3, db = b & 7;
  const int t0 = tb * 32, d0 = db * 64;
  const int oct = tid & 7, r = tid >> 3;
  const int t = t0 + r;
  uint4 kk[4], vv, sk[4];
#pragma unroll
  for (int i = 0; i < 4; ++i) {
    int tt = t - 3 + i;
    kk[i] = (tt >= 0) ? *(const uint4*)(kin + (size_t)tt * D_DIM + d0 + oct * 8) : make_uint4(0, 0, 0, 0);
  }
  vv = *(const uint4*)(vin + (size_t)t * D_DIM + d0 + oct * 8);
#pragma unroll
  for (int i = 0; i < 4; ++i) sk[i] = *(const uint4*)(shift_k + (size_t)(d0 + oct * 8) * 4 + i * 8);
  const u32* kw = (const u32*)kk;
  const u32* vw = (const u32*)&vv;
  const u32* sw = (const u32*)sk;
#pragma unroll
  for (int c = 0; c < 8; ++c) {
    float w[4];
    w[0] = lo_f(sw[c * 2]); w[1] = hi_f(sw[c * 2]); w[2] = lo_f(sw[c * 2 + 1]); w[3] = hi_f(sw[c * 2 + 1]);
    float acc = 0.f;
#pragma unroll
    for (int tp = 0; tp < 4; ++tp) {
      u32 kwv = kw[tp * 4 + (c >> 1)];
      float kval = (c & 1) ? hi_f(kwv) : lo_f(kwv);
      float prod = rbf(kval * w[3 - tp]);
      acc = rbf(acc + prod);
    }
    float vval = (c & 1) ? hi_f(vw[c >> 1]) : lo_f(vw[c >> 1]);
    T[(oct * 8 + c) * 34 + r] = f2bf(acc * vval);
  }
  __syncthreads();
  const int hw = tid >> 5, l32 = tid & 31;
#pragma unroll
  for (int c = 0; c < 8; ++c) {
    int dd = hw * 8 + c;
    G[(size_t)(d0 + dd) * G_STRIDE + (2079 - (t0 + l32))] = T[dd * 34 + l32];
  }
  if (tb == 0) {
#pragma unroll
    for (int c = 0; c < 8; ++c) G[(size_t)(d0 + hw * 8 + c) * G_STRIDE + 2080 + l32] = 0;
  }
  if (tb == 63) {
#pragma unroll
    for (int c = 0; c < 8; ++c) G[(size_t)(d0 + hw * 8 + c) * G_STRIDE + l32] = 0;
  }
}

#define KC_BYTES 4416
#define HI_BYTES 5408
#define CH_BYTES (8 * KC_BYTES + 4 * HI_BYTES)
#define RT_OFF (2 * CH_BYTES)
#define CONV_LDS (RT_OFF + 2 * 3 * 32 * 72 * 2)
#define W1_UNITS (16 * 22)
#define W2_UNITS (43 * 8)

#define CONV_PD 4
__device__ __forceinline__ constexpr int conv_nt(int s) { return 4 - (s >= 17) - (s >= 33) - (s >= 49); }

template <int S0, int S1>
__device__ __forceinline__ void conv_range(const unsigned char* kcb, const unsigned char* hib, f32x4 (&acc)[4][2]) {
  uint4 bb[CONV_PD][2], aa[CONV_PD][4];
#pragma unroll
  for (int p = 0; p < CONV_PD - 1; ++p) {
    const int s = S0 + p;
    if (s < S1) {
#pragma unroll
      for (int nt = 0; nt < 2; ++nt) bb[p][nt] = *(const uint4*)(kcb - (16 * nt + 32 * s) * 2);
#pragma unroll
      for (int a = 0; a < 4; ++a)
        if (a < conv_nt(s)) aa[p][a] = *(const uint4*)(hib + (512 * (3 - a) - 32 * s) * 2);
    }
  }
  __builtin_amdgcn_sched_barrier(0);
#pragma unroll
  for (int s = S0; s < S1; ++s) {
    const int sn = s + CONV_PD - 1;
    if (sn < S1) {
      const int p = (sn - S0) % CONV_PD;
#pragma unroll
      for (int nt = 0; nt < 2; ++nt) bb[p][nt] = *(const uint4*)(kcb - (16 * nt + 32 * sn) * 2);
#pragma unroll
      for (int a = 0; a < 4; ++a)
        if (a < conv_nt(sn)) aa[p][a] = *(const uint4*)(hib + (512 * (3 - a) - 32 * sn) * 2);
    }
    __builtin_amdgcn_sched_barrier(0);
    const int p = (s - S0) % CONV_PD;
#pragma unroll
    for (int a = 0; a < 4; ++a)
      if (a < conv_nt(s)) {
#pragma unroll
        for (int nt = 0; nt < 2; ++nt)
          acc[3 - a][nt] = __builtin_amdgcn_mfma_f32_16x16x32_bf16(as_frag(bb[p][nt]), as_frag(aa[p][a]), acc[3 - a][nt], 0, 0, 0);
      }
    __builtin_amdgcn_sched_barrier(0);
  }
}

__global__ __launch_bounds__(512) void k_conv(const u16* __restrict__ G, const u16* __restrict__ ssm_k,
                                              const u16* __restrict__ ssm_D, u16* __restrict__ y,
                                              const u16* __restrict__ wg, const u16* __restrict__ wu,
                                              const u16* __restrict__ wd, u16* __restrict__ Wt,
                                              u16* __restrict__ Wdt) {
  __shared__ __attribute__((aligned(16))) unsigned char smem[CONV_LDS];
  const int tid = threadIdx.x;
  const int ch = tid >> 8, t256 = tid & 255;
  const int pb = (blockIdx.x & 7) * 32 + (blockIdx.x >> 3);
  const int d = pb * 2 + ch;
  unsigned char* base = smem + ch * CH_BYTES;
  unsigned char* kc = base;
  unsigned char* hi = base + 8 * KC_BYTES;

  const uint4* gsrc = (const uint4*)(G + (size_t)d * G_STRIDE);
  const uint4* hsrc = (const uint4*)(ssm_k + (size_t)d * S_LEN);
  uint4 g0 = gsrc[t256], g1 = gsrc[t256 + 1];
  uint4 g2 = make_uint4(0, 0, 0, 0), g3 = make_uint4(0, 0, 0, 0);
  if (t256 < 7) { g2 = gsrc[256 + t256]; g3 = gsrc[257 + t256]; }
  uint4 h0 = hsrc[t256];

  const int rrow = t256 >> 3, roct = t256 & 7;
  uint4 wl[2][2];
#pragma unroll
  for (int rnd = 0; rnd < 2; ++rnd) {
    const int u = blockIdx.x + rnd * 256;
    wl[rnd][0] = make_uint4(0, 0, 0, 0); wl[rnd][1] = make_uint4(0, 0, 0, 0);
    if (ch == 0) {
      const int ks = u / 22, fb = u % 22;
      const int f = fb * 64 + roct * 8;
      if (u < W1_UNITS && f < F_DIM) {
        wl[rnd][0] = *(const uint4*)(wg + (size_t)(ks * 32 + rrow) * F_DIM + f);
        wl[rnd][1] = *(const uint4*)(wu + (size_t)(ks * 32 + rrow) * F_DIM + f);
      }
    } else {
      if (u < W2_UNITS) {
        const int ks = u >> 3, nb = u & 7;
        wl[rnd][0] = *(const uint4*)(wd + (size_t)(ks * 32 + rrow) * D_DIM + nb * 64 + roct * 8);
      }
    }
  }

#pragma unroll
  for (int rep = 0; rep < 2; ++rep) {
    const int q = t256 + rep * 256;
    if (rep == 0 || t256 < 7) {
      uint4 lo = rep ? g2 : g0, hh = rep ? g3 : g1;
      u32 W[8] = {lo.x, lo.y, lo.z, lo.w, hh.x, hh.y, hh.z, hh.w};
#pragma unroll
      for (int e = 0; e < 8; ++e) {
        const int sft = 7 - e;
        u32 o[4];
#pragma unroll
        for (int j = 0; j < 4; ++j) {
          if (sft & 1) o[j] = __builtin_amdgcn_alignbit(W[(sft >> 1) + j + 1], W[(sft >> 1) + j], 16);
          else o[j] = W[(sft >> 1) + j];
        }
        *(uint4*)(kc + e * KC_BYTES + q * 16) = make_uint4(o[0], o[1], o[2], o[3]);
      }
    }
  }
  {
#pragma unroll
    for (int c = 0; c < 4; ++c) *(uint4*)(hi + c * HI_BYTES + (64 + t256) * 16) = h0;
    if (t256 < 68) {
      const int q = (t256 < 64) ? t256 : (256 + t256);
#pragma unroll
      for (int c = 0; c < 4; ++c) *(uint4*)(hi + c * HI_BYTES + q * 16) = make_uint4(0, 0, 0, 0);
    }
  }
  __syncthreads();
  const int l = tid & 63, wq = (tid >> 6) & 3;
  const int c16 = l & 15, hh = l >> 4;
  const unsigned char* kcb = kc + (c16 & 7) * KC_BYTES + (2072 + 8 * hh - (c16 & 8)) * 2;
  const unsigned char* hib = hi + (c16 >> 2) * HI_BYTES + (512 + 32 * c16 + 8 * hh) * 2;
  f32x4 acc[4][2];
#pragma unroll
  for (int a = 0; a < 4; ++a)
#pragma unroll
    for (int nt = 0; nt < 2; ++nt) acc[a][nt] = (f32x4){0.f, 0.f, 0.f, 0.f};

  {
    const int wqu = __builtin_amdgcn_readfirstlane(wq);
    if (wqu == 0) conv_range<0, 10>(kcb, hib, acc);
    else if (wqu == 1) conv_range<10, 21>(kcb, hib, acc);
    else if (wqu == 2) conv_range<21, 36>(kcb, hib, acc);
    else conv_range<36, 65>(kcb, hib, acc);
  }

  u16* sc = (u16*)(smem + RT_OFF);
#pragma unroll
  for (int rnd = 0; rnd < 2; ++rnd) {
    u16* scr = sc + rnd * (3 * 32 * 72);
    if (ch == 0) {
      *(uint4*)(scr + rrow * 72 + roct * 8) = wl[rnd][0];
      *(uint4*)(scr + 32 * 72 + rrow * 72 + roct * 8) = wl[rnd][1];
    } else {
      *(uint4*)(scr + 64 * 72 + rrow * 72 + roct * 8) = wl[rnd][0];
    }
  }
  __syncthreads();
  float* red = (float*)(base + KC_BYTES);
#pragma unroll
  for (int a = 0; a < 4; ++a)
#pragma unroll
    for (int nt = 0; nt < 2; ++nt)
      *(f32x4*)(red + wq * 2304 + (16 * a + c16) * 36 + nt * 16 + 4 * hh) = acc[a][nt];
  __syncthreads();
#pragma unroll
  for (int rnd = 0; rnd < 2; ++rnd) {
    const int u = blockIdx.x + rnd * 256;
    const u16* scr = sc + rnd * (3 * 32 * 72);
    if (ch == 0) {
      if (u < W1_UNITS) {
        const int ks = u / 22, fb = u % 22;
#pragma unroll
        for (int rep = 0; rep < 2; ++rep) {
          int it = t256 + rep * 256;
          int tl = it >> 6, l = it & 63;
          int tile = fb * 8 + tl;
          if (tile < NFT) {
            int rr = l & 15;
            int fl = tl * 8 + (rr >> 1);
            const u16* src = scr + (rr & 1) * 32 * 72;
            int kq = (l >> 4) * 8;
            u32 o[4];
#pragma unroll
            for (int j = 0; j < 4; ++j)
              o[j] = (u32)src[(kq + 2 * j) * 72 + fl] | ((u32)src[(kq + 2 * j + 1) * 72 + fl] << 16);
            *(uint4*)(Wt + ((size_t)(tile * NKS1 + ks) * 64 + l) * 8) = make_uint4(o[0], o[1], o[2], o[3]);
          }
        }
      }
    } else {
      if (u < W2_UNITS) {
        const int ks = u >> 3, nb = u & 7;
        const u16* src = scr + 64 * 72;
        int nt = t256 >> 6, l = t256 & 63;
        int n = nt * 16 + (l & 15);
        int kq = (l >> 4) * 8;
        u32 o[4];
#pragma unroll
        for (int j = 0; j < 4; ++j)
          o[j] = (u32)src[(kq + 2 * j) * 72 + n] | ((u32)src[(kq + 2 * j + 1) * 72 + n] << 16);
        *(uint4*)(Wdt + ((size_t)((nb * 4 + nt) * NKS2 + ks) * 64 + l) * 8) = make_uint4(o[0], o[1], o[2], o[3]);
      }
    }
  }
  {
    const int t = tid * 4;
    const int ro = (t >> 5) * 36 + (t & 31);
    float res[2][4];
#pragma unroll
    for (int cc = 0; cc < 2; ++cc) {
      const unsigned char* bb = smem + cc * CH_BYTES;
      const float* rd = (const float*)(bb + KC_BYTES);
      f32x4 s = *(const f32x4*)(rd + ro);
#pragma unroll
      for (int w = 1; w < 4; ++w) s += *(const f32x4*)(rd + w * 2304 + ro);
      float Dv = bf2f(ssm_D[pb * 2 + cc]);
      const u16* k0 = (const u16*)bb;
#pragma unroll
      for (int r = 0; r < 4; ++r) {
        float kvv = bf2f(k0[2072 - (t + r)]);
        res[cc][r] = rbf(s[r]) + rbf(kvv * Dv);
      }
    }
#pragma unroll
    for (int r = 0; r < 4; ++r)
      *(u32*)(y + (size_t)(t + r) * D_DIM + pb * 2) = pack2(res[0][r], res[1][r]);
  }
}

template <int CTRL>
__device__ __forceinline__ float dpp_add(float x) {
  int v = __builtin_amdgcn_update_dpp(0, __float_as_int(x), CTRL, 0xf, 0xf, true);
  return x + __int_as_float(v);
}
__device__ __forceinline__ float row16_sum(float x) {
  x = dpp_add<0xB1>(x);
  x = dpp_add<0x4E>(x);
  x = dpp_add<0x141>(x);
  x = dpp_add<0x140>(x);
  return x;
}

typedef __attribute__((ext_vector_type(2))) float f32x2;
typedef __attribute__((ext_vector_type(2))) __bf16 bf16x2_t;
__device__ __forceinline__ u32 cvt_pk(float a, float b) {
  f32x2 v = {a, b};
  bf16x2_t r = __builtin_convertvector(v, bf16x2_t);
  return __builtin_bit_cast(u32, r);
}
__device__ __forceinline__ void ln_stats(const u32 (&xp)[16], float& inv, float& nmi) {
  const bf16x2_t one = __builtin_bit_cast(bf16x2_t, 0x3f803f80u);
  float s0 = 0.f, s1 = 0.f, q0 = 0.f, q1 = 0.f;
#pragma unroll
  for (int i = 0; i < 16; i += 2) {
    bf16x2_t a = __builtin_bit_cast(bf16x2_t, xp[i]), b = __builtin_bit_cast(bf16x2_t, xp[i + 1]);
    s0 = __builtin_amdgcn_fdot2_f32_bf16(a, one, s0, false);
    q0 = __builtin_amdgcn_fdot2_f32_bf16(a, a, q0, false);
    s1 = __builtin_amdgcn_fdot2_f32_bf16(b, one, s1, false);
    q1 = __builtin_amdgcn_fdot2_f32_bf16(b, b, q1, false);
  }
  float mean = row16_sum(s0 + s1) * (1.0f / 512.0f);
  float ex2 = row16_sum(q0 + q1) * (1.0f / 512.0f);
  float var = rbf(fmaxf(ex2 - mean * mean, 0.f));
  float den = rbf(__builtin_amdgcn_sqrtf(rbf(var + 1e-5f)));
  inv = __builtin_amdgcn_rcpf(den);
  nmi = -rbf(mean) * inv;
}

#define FFN1_PD 6
#define AUX_SC1 16
#define RSRC_W3 0x00020000
typedef unsigned v4u __attribute__((ext_vector_type(4)));
typedef __attribute__((address_space(1))) unsigned gu32;
#define RLX_AGENT __ATOMIC_RELAXED, __HIP_MEMORY_SCOPE_AGENT
#define SPIN_CAP (1u << 20)

__device__ __forceinline__ uint4 ld_sc1(__amdgpu_buffer_rsrc_t rs, int off) {
  v4u v = __builtin_amdgcn_raw_buffer_load_b128(rs, off, 0, AUX_SC1);
  return make_uint4(v.x, v.y, v.z, v.w);
}
__device__ __forceinline__ void st_sc1(__amdgpu_buffer_rsrc_t rs, int off, uint4 v) {
  v4u t = {v.x, v.y, v.z, v.w};
  __builtin_amdgcn_raw_buffer_store_b128(t, rs, off, 0, AUX_SC1);
}
__device__ __forceinline__ void publish(gu32* cnt) {
  asm volatile("s_waitcnt vmcnt(0)" ::: "memory");
  __syncthreads();
  if (threadIdx.x == 0) __hip_atomic_fetch_add(cnt, 1u, RLX_AGENT);
}
__device__ __forceinline__ void wait_for(gu32* cnt, unsigned want) {
  if (threadIdx.x == 0) {
    for (unsigned spins = 0; spins < SPIN_CAP; ++spins) {
      if (__hip_atomic_load(cnt, RLX_AGENT) >= want) break;
      __builtin_amdgcn_s_sleep(1);
    }
    __builtin_amdgcn_fence(__ATOMIC_ACQUIRE, "wavefront");
  }
  __syncthreads();
}

#define XS_BYTES 65536
#define RED_BYTES (8 * 64 * 68 * 4)
#define XRES_OFF RED_BYTES
#define FFN_LDS (RED_BYTES + 64 * 144)

template <int NT, bool XSC1>
__device__ __forceinline__ void ffn_phase_a(const u16* __restrict__ xin, __amdgpu_buffer_rsrc_t rsx,
                                            const uint4 (&yr)[2][4], const u16* __restrict__ Wt,
                                            __amdgpu_buffer_rsrc_t rsh, unsigned char* lds, int rb, int fg, int tile0,
                                            gu32* xcnt) {
  const int tid = threadIdx.x;
  const int w = tid >> 6, l = tid & 63;
  const int rr = l >> 4, sub = l & 15;
  unsigned char* xs = lds;

  const uint4* wbase[NT];
#pragma unroll
  for (int i = 0; i < NT; ++i) wbase[i] = (const uint4*)(Wt) + (size_t)(tile0 + w + 8 * i) * (NKS1 * 64) + l;
  uint4 wf[FFN1_PD][NT];
  uint4 xr[2][4];
  if (XSC1) wait_for(xcnt, 8);
#pragma unroll
  for (int p = 0; p < 2; ++p) {
    const int rowoff = (rb * 64 + w * 8 + p * 4 + rr) * D_DIM + sub * 8;
#pragma unroll
    for (int c = 0; c < 4; ++c) {
      if (XSC1) xr[p][c] = ld_sc1(rsx, (rowoff + c * 128) * 2);
      else xr[p][c] = *(const uint4*)(xin + rowoff + c * 128);
    }
  }
  __builtin_amdgcn_sched_barrier(0);
#pragma unroll
  for (int s = 0; s < 2; ++s)
#pragma unroll
    for (int i = 0; i < NT; ++i) wf[s][i] = wbase[i][s * 64];
  __builtin_amdgcn_sched_barrier(0);
#pragma unroll
  for (int p = 0; p < 2; ++p) {
    const int row = w * 8 + p * 4 + rr;
    u32 xp[16];
#pragma unroll
    for (int c = 0; c < 4; ++c) { xp[c * 4] = xr[p][c].x; xp[c * 4 + 1] = xr[p][c].y; xp[c * 4 + 2] = xr[p][c].z; xp[c * 4 + 3] = xr[p][c].w; }
    float inv, nmi;
    ln_stats(xp, inv, nmi);
    f32x2 inv2 = {inv, inv}, nmi2 = {nmi, nmi};
    f32x2 rv[16];
#pragma unroll
    for (int c = 0; c < 4; ++c) {
      const u32* yw = (const u32*)&yr[p][c];
#pragma unroll
      for (int j = 0; j < 4; ++j) {
        f32x2 xv = {lo_f(xp[c * 4 + j]), hi_f(xp[c * 4 + j])};
        f32x2 yv = {lo_f(yw[j]), hi_f(yw[j])};
        f32x2 ln = xv * inv2 + nmi2;
        rv[c * 4 + j] = ln * yv + xv;
        xp[c * 4 + j] = cvt_pk(rv[c * 4 + j].x, rv[c * 4 + j].y);
      }
    }
    {
      const int cc = fg >> 1, s0 = (fg & 1) * 8;
      if (sub >= s0 && sub < s0 + 8) {
        uint4 o = make_uint4(0, 0, 0, 0);
#pragma unroll
        for (int c = 0; c < 4; ++c)
          if (c == cc) o = make_uint4(xp[c * 4], xp[c * 4 + 1], xp[c * 4 + 2], xp[c * 4 + 3]);
        *(uint4*)(lds + XRES_OFF + row * 144 + (sub - s0) * 16) = o;
      }
    }
    ln_stats(xp, inv, nmi);
    inv2 = (f32x2){inv, inv}; nmi2 = (f32x2){nmi, nmi};
    const int ntile = row >> 4, tok = row & 15;
#pragma unroll
    for (int c = 0; c < 4; ++c) {
      u32 o[4];
#pragma unroll
      for (int j = 0; j < 4; ++j) { f32x2 r = rv[c * 4 + j] * inv2 + nmi2; o[j] = cvt_pk(r.x, r.y); }
      const int kstep = c * 4 + (sub >> 2), chunk = sub & 3;
      *(uint4*)(xs + ((ntile * 16 + kstep) * 64 + chunk * 16 + (tok ^ sub)) * 16) = make_uint4(o[0], o[1], o[2], o[3]);
    }
    __builtin_amdgcn_sched_barrier(0);
#pragma unroll
    for (int s = 2 + 2 * p; s < 4 + 2 * p; ++s)
#pragma unroll
      for (int i = 0; i < NT; ++i) wf[s][i] = wbase[i][s * 64];
    __builtin_amdgcn_sched_barrier(0);
  }
  __syncthreads();
  f32x4 acc[NT][4];
#pragma unroll
  for (int i = 0; i < NT; ++i)
#pragma unroll
    for (int n = 0; n < 4; ++n) acc[i][n] = (f32x4){0.f, 0.f, 0.f, 0.f};
  uint4 xf[2][4];
#pragma unroll
  for (int n = 0; n < 4; ++n) xf[0][n] = *(const uint4*)(xs + ((n * 16) * 64 + rr * 16 + (sub ^ rr)) * 16);
  __builtin_amdgcn_sched_barrier(0);
#pragma unroll
  for (int s = 0; s < NKS1; ++s) {
    if (s + 1 < NKS1) {
      const int swz = sub ^ (((s + 1) * 4 + rr) & 15);
#pragma unroll
      for (int n = 0; n < 4; ++n) xf[(s + 1) & 1][n] = *(const uint4*)(xs + ((n * 16 + s + 1) * 64 + rr * 16 + swz) * 16);
    }
    __builtin_amdgcn_sched_barrier(0);
#pragma unroll
    for (int i = 0; i < NT; ++i)
#pragma unroll
      for (int n = 0; n < 4; ++n)
        acc[i][n] = __builtin_amdgcn_mfma_f32_16x16x32_bf16(as_frag(wf[s % FFN1_PD][i]), as_frag(xf[s & 1][n]), acc[i][n], 0, 0, 0);
    __builtin_amdgcn_sched_barrier(0);
    if (s + FFN1_PD < NKS1) {
#pragma unroll
      for (int i = 0; i < NT; ++i) wf[s % FFN1_PD][i] = wbase[i][(s + FFN1_PD) * 64];
    }
    __builtin_amdgcn_sched_barrier(0);
  }
#pragma unroll
  for (int i = 0; i < NT; ++i) {
    const int ft = tile0 + w + 8 * i;
    u32 r[4];
#pragma unroll
    for (int n = 0; n < 4; ++n) {
      float hv[2];
#pragma unroll
      for (int e = 0; e < 2; ++e) {
        float g = rbf(acc[i][n][2 * e]);
        float u = rbf(acc[i][n][2 * e + 1]);
        hv[e] = rbf(g * __builtin_amdgcn_rcpf(1.0f + __expf(-g))) * u;
      }
      r[n] = cvt_pk(hv[0], hv[1]);
    }
    {
      auto t0 = __builtin_amdgcn_permlane32_swap(r[0], r[2], false, false); r[0] = t0[0]; r[2] = t0[1];
      auto t1 = __builtin_amdgcn_permlane32_swap(r[1], r[3], false, false); r[1] = t1[0]; r[3] = t1[1];
      auto t2 = __builtin_amdgcn_permlane16_swap(r[0], r[1], false, false); r[0] = t2[0]; r[1] = t2[1];
      auto t3 = __builtin_amdgcn_permlane16_swap(r[2], r[3], false, false); r[2] = t3[0]; r[3] = t3[1];
    }
    st_sc1(rsh, (((rb * 4 + rr) * NFT + ft) * 16 + sub) * 16, make_uint4(r[0], r[1], r[2], r[3]));
  }
}

#define FFN2_HPD 2
template <int NS>
__device__ __forceinline__ void ffn_phase_b(const uint4 (&wf)[6][4], __amdgpu_buffer_rsrc_t rsh, int hoff,
                                            f32x4 (&acc)[4][4]) {
  uint4 hf[FFN2_HPD][4];
#pragma unroll
  for (int s = 0; s < FFN2_HPD; ++s)
#pragma unroll
    for (int i = 0; i < 4; ++i) hf[s][i] = ld_sc1(rsh, hoff + i * (NFT * 256) + s * 1024);
  __builtin_amdgcn_sched_barrier(0);
#pragma unroll
  for (int s = 0; s < NS; ++s) {
#pragma unroll
    for (int m = 0; m < 4; ++m)
#pragma unroll
      for (int n = 0; n < 4; ++n)
        acc[m][n] = __builtin_amdgcn_mfma_f32_16x16x32_bf16(as_frag(wf[s][m]), as_frag(hf[s % FFN2_HPD][n]), acc[m][n], 0, 0, 0);
    __builtin_amdgcn_sched_barrier(0);
    if (s + FFN2_HPD < NS) {
#pragma unroll
      for (int i = 0; i < 4; ++i) hf[s % FFN2_HPD][i] = ld_sc1(rsh, hoff + i * (NFT * 256) + (s + FFN2_HPD) * 1024);
    }
    __builtin_amdgcn_sched_barrier(0);
  }
}

template <int LAYER>
__device__ __forceinline__ void ffn_layer(const u16* q, const u16* y, const u16* Wt, const u16* Wdt, u16* hT,
                                          u16* Xb, u16* out, gu32* cnt, unsigned char* lds, int rb, int fg) {
  asm volatile("" : "+s"(y), "+s"(Wt), "+s"(Wdt));
  const int tile0 = (fg < 4) ? 22 * fg : 88 + 21 * (fg - 4);
  const int ntiles = (fg < 4) ? 22 : 21;
  const int tid = threadIdx.x;
  const int w = __builtin_amdgcn_readfirstlane(tid >> 6), l = tid & 63;
  const int rr = l >> 4, sub = l & 15;
  const __amdgpu_buffer_rsrc_t rsx = __builtin_amdgcn_make_buffer_rsrc((void*)Xb, 0, S_LEN * D_DIM * 2, RSRC_W3);
  const __amdgpu_buffer_rsrc_t rsh =
      __builtin_amdgcn_make_buffer_rsrc((void*)(hT + (size_t)LAYER * (128 * NFT * 128)), 0, 128 * NFT * 256, RSRC_W3);
  uint4 yr[2][4];
#pragma unroll
  for (int p = 0; p < 2; ++p) {
    const int rowoff = (rb * 64 + w * 8 + p * 4 + rr) * D_DIM + sub * 8;
#pragma unroll
    for (int c = 0; c < 4; ++c) yr[p][c] = *(const uint4*)(y + rowoff + c * 128);
  }
  __builtin_amdgcn_sched_barrier(0);
  if (w + 16 < ntiles) ffn_phase_a<3, LAYER == 1>(q, rsx, yr, Wt, rsh, lds, rb, fg, tile0, cnt + 1);
  else ffn_phase_a<2, LAYER == 1>(q, rsx, yr, Wt, rsh, lds, rb, fg, tile0, cnt + 1);
  publish(cnt + LAYER * 2);
  const int ks0 = w * 5 + min(w, 3);
  uint4 wf[6][4];
  {
    const uint4* wp = (const uint4*)Wdt + (size_t)(fg * 4) * (NKS2 * 64) + ks0 * 64 + l;
#pragma unroll
    for (int s = 0; s < 6; ++s)
#pragma unroll
      for (int i = 0; i < 4; ++i)
        wf[s][i] = wp[(size_t)i * (NKS2 * 64) + min(s, (w < 3) ? 5 : 4) * 64];
  }
  __builtin_amdgcn_sched_barrier(0);
  wait_for(cnt + LAYER * 2, 8);
  f32x4 acc[4][4];
#pragma unroll
  for (int m = 0; m < 4; ++m)
#pragma unroll
    for (int n = 0; n < 4; ++n) acc[m][n] = (f32x4){0.f, 0.f, 0.f, 0.f};
  const int hoff = (rb * 4) * (NFT * 256) + ks0 * 1024 + l * 16;
  if (w < 3) ffn_phase_b<6>(wf, rsh, hoff, acc);
  else ffn_phase_b<5>(wf, rsh, hoff, acc);
  float* red = (float*)lds;
#pragma unroll
  for (int m = 0; m < 4; ++m)
#pragma unroll
    for (int n = 0; n < 4; ++n)
      *(f32x4*)(red + (w * 64 + n * 16 + sub) * 68 + m * 16 + 4 * rr) = acc[m][n];
  __syncthreads();
  {
    const int tok = tid >> 3, no = tid & 7;
    f32x4 s0 = *(const f32x4*)(red + tok * 68 + no * 8), s1 = *(const f32x4*)(red + tok * 68 + no * 8 + 4);
#pragma unroll
    for (int ww = 1; ww < 8; ++ww) {
      s0 += *(const f32x4*)(red + (ww * 64 + tok) * 68 + no * 8);
      s1 += *(const f32x4*)(red + (ww * 64 + tok) * 68 + no * 8 + 4);
    }
    const uint4 rx = *(const uint4*)(lds + XRES_OFF + tok * 144 + no * 16);
    uint4 o;
    o.x = cvt_pk(lo_f(rx.x) + rbf(s0[0]), hi_f(rx.x) + rbf(s0[1]));
    o.y = cvt_pk(lo_f(rx.y) + rbf(s0[2]), hi_f(rx.y) + rbf(s0[3]));
    o.z = cvt_pk(lo_f(rx.z) + rbf(s1[0]), hi_f(rx.z) + rbf(s1[1]));
    o.w = cvt_pk(lo_f(rx.w) + rbf(s1[2]), hi_f(rx.w) + rbf(s1[3]));
    const int ooff = (rb * 64 + tok) * D_DIM + fg * 64 + no * 8;
    if (LAYER == 0) {
      st_sc1(rsx, ooff * 2, o);
      publish(cnt + 1);
    } else {
      *(uint4*)(out + ooff) = o;
    }
  }
}

__global__ __launch_bounds__(512, 2) void k_ffn(const u16* __restrict__ q, const u16* __restrict__ y,
                                                const u16* __restrict__ Wt, const u16* __restrict__ Wdt,
                                                u16* __restrict__ hT, u16* __restrict__ Xb,
                                                u16* __restrict__ out, unsigned* __restrict__ cnt_raw) {
  __shared__ __attribute__((aligned(16))) unsigned char lds[FFN_LDS];
  const int b = blockIdx.x;
  const int rb = (b & 7) * 4 + ((b >> 3) & 3);
  const int fg = b >> 5;
  gu32* cnt = (gu32*)cnt_raw + rb * 32;
  ffn_layer<0>(q, y, Wt, Wdt, hT, Xb, out, cnt, lds, rb, fg);
  ffn_layer<1>(q, y, Wt, Wdt, hT, Xb, out, cnt, lds, rb, fg);
}

extern "C" void kernel_launch(void* const* d_in, const int* in_sizes, int n_in, void* d_out, int out_size,
                              void* d_ws, size_t ws_size, hipStream_t stream) {
  const u16* q = (const u16*)d_in[0];
  const u16* k = (const u16*)d_in[1];
  const u16* v = (const u16*)d_in[2];
  const u16* shift_k = (const u16*)d_in[3];
  const u16* ssm_k = (const u16*)d_in[4];
  const u16* ssm_D = (const u16*)d_in[5];
  const u16* wg = (const u16*)d_in[7];
  const u16* wu = (const u16*)d_in[8];
  const u16* wd = (const u16*)d_in[9];
  u16* out = (u16*)d_out;

  unsigned char* ws = (unsigned char*)d_ws;
  size_t off = 0;
  u16* G = (u16*)(ws + off);   off += (size_t)D_DIM * G_STRIDE * 2;
  u16* Y = (u16*)(ws + off);   off += (size_t)S_LEN * D_DIM * 2;
  u16* Wt = (u16*)(ws + off);  off += (size_t)NFT * NKS1 * 1024;
  u16* Wdt = (u16*)(ws + off); off += (size_t)32 * NKS2 * 1024;
  u16* hT = (u16*)(ws + off);  off += (size_t)2 * 128 * NFT * 256;
  u16* Xb = (u16*)(ws + off);  off += (size_t)S_LEN * D_DIM * 2;
  unsigned* cnt = (unsigned*)(ws + off); off += 32 * 128;

  k_prep<<<512, 256, 0, stream>>>(k, v, shift_k, G, cnt);
  k_conv<<<256, 512, 0, stream>>>(G, ssm_k, ssm_D, Y, wg, wu, wd, Wt, Wdt);
  k_ffn<<<256, 512, 0, stream>>>(q, Y, Wt, Wdt, hT, Xb, out, cnt);
}
```
